```python
import torch

def _softmax(x, axis=-1):
    return torch.softmax(x, dim=axis)
def _gelu(x):
    return torch.nn.functional.gelu(x, approximate="tanh")
def _silu(x):
    return torch.nn.functional.silu(x)
def _sigmoid(x):
    return torch.sigmoid(x)
def _layernorm(x, axis=-1, eps=1e-5):
    m = x.mean(dim=axis, keepdim=True)
    v = x.var(dim=axis, keepdim=True, unbiased=False)
    return (x - m) / torch.sqrt(v + eps)
def _scatter_add(tgt, idx, src, axis=0):
    return tgt.clone().index_add_(axis, idx, src)
def _segment_sum(src, seg, n):
    out = torch.zeros((n,) + src.shape[1:], dtype=src.dtype)
    return out.index_add_(0, seg, src)
def _pad(x, pw):
    flat = tuple(v for pair in reversed(pw) for v in pair)
    return torch.nn.functional.pad(x, flat)

kernel_name = 'gated_mix__spectral_gfnet__mixer_token_mlp__prompt'

def setup_inputs():

    _w = (lambda a: (lambda t: t.to(torch.bfloat16) if t.is_floating_point() else t)(torch.as_tensor(a)))
    def _A_setup():
        B, S, D = 1, 2048, 512
        Df = D // 2 + 1
        return {
            "x": _w((lambda *s: torch.randn(*s, dtype=torch.bfloat16) * 0.05)(B, S, D)),
            "w_r": _w((lambda *s: torch.randn(*s, dtype=torch.bfloat16) * 0.8)(S, Df) * 0.02),
            "w_i": _w((lambda *s: torch.randn(*s, dtype=torch.bfloat16) * 1.6)(S, Df) * 0.02),
        }
    _A_in = _A_setup()

    def _B_setup():
        B, S, D, F = 1, 2048, 512, 1792
        return {
            "x": _w((lambda *s: torch.randn(*s, dtype=torch.bfloat16) * 0.05)(B, S, D)),
            "w1": _w((lambda *s: torch.randn(*s, dtype=torch.bfloat16) * 0.2)(S, F) * (S ** -0.5)),
            "b1": _w((lambda *s: torch.randn(*s, dtype=torch.bfloat16) * 3.2)(F) * 0.02),
            "w2": _w((lambda *s: torch.randn(*s, dtype=torch.bfloat16) * 0.05)(F, S) * (F ** -0.5)),
            "b2": _w((lambda *s: torch.randn(*s, dtype=torch.bfloat16) * 0.1)(S) * 0.02),
        }
    _B_in = _B_setup()

    return {
        "A_x": _A_in["x"],
        "A_w_r": _A_in["w_r"],
        "A_w_i": _A_in["w_i"],
        "B_x": _B_in["x"],
        "B_w1": _B_in["w1"],
        "B_b1": _B_in["b1"],
        "B_w2": _B_in["w2"],
        "B_b2": _B_in["b2"],
        "gate_w": _w((lambda *s: torch.randn(*s, dtype=torch.bfloat16) * 0.05)(512) * 16.0),
    }

def reference(A_x, A_w_r, A_w_i, B_x, B_w1, B_b1, B_w2, B_b2, gate_w):
    def _A_ref(x, w_r, w_i):
        B, S, D = x.shape
        X = (lambda x, axes=(-2,-1): torch.fft.rfft2(x.float(), dim=axes))(x, axes=(-2, -1))
        W = w_r + 1j * w_i
        X = X * W
        y = (lambda x, s, axes=(-2,-1): torch.fft.irfft2(x, s=s, dim=axes).to(torch.bfloat16))(X, (S, D), axes=(-2, -1))
        return y

    def _B_ref(x, w1, b1, w2, b2):
        B, S, D = x.shape
        h = _layernorm(x, axis=-1)
        u = torch.einsum("bsd,sf->bdf", h, w1) + b1
        u = _gelu(u)
        y = torch.einsum("bdf,fs->bds", u, w2) + b2
        return (lambda x, *d: x.permute(*d))(y, 0, 2, 1)

    x = A_x
    g = _sigmoid((x * gate_w).sum(-1, keepdims=True))
    a = _A_ref(x, A_w_r, A_w_i)
    b = _B_ref(x, B_w1, B_b1, B_w2, B_b2)
    return g * a + (1 - g) * b
```

```cpp
#include <hip/hip_runtime.h>
#include <stdint.h>

typedef unsigned short u16;
typedef unsigned int u32;
typedef __attribute__((ext_vector_type(2))) float f2;
typedef __attribute__((ext_vector_type(4))) float f4;
typedef __attribute__((ext_vector_type(16))) float f16v;
typedef __attribute__((ext_vector_type(2))) __bf16 bf2;
typedef __attribute__((ext_vector_type(4))) __bf16 bf4;
typedef __attribute__((ext_vector_type(8))) __bf16 bf8;
typedef __attribute__((ext_vector_type(2))) u32 u2;
typedef __attribute__((ext_vector_type(4))) u32 u4;
typedef __attribute__((ext_vector_type(4))) short s4;
#define LAS __attribute__((address_space(3)))
#define DI __device__ __forceinline__

constexpr int S_ = 2048, D_ = 512, F_ = 1792, DF_ = 257;

constexpr size_t OFF_HP = 0;
constexpr size_t OFF_T = OFF_HP + 2097152;
constexpr size_t OFF_WT = OFF_T + 257 * 2048 * 8;
constexpr size_t OFF_G = OFF_WT + 257 * 2048 * 4;
constexpr size_t OFF_UP = OFF_G + 8192;
constexpr size_t OFF_YT = OFF_UP + 144 * 16384;

DI float bflo(u32 v) { return __uint_as_float(v << 16); }
DI float bfhi(u32 v) { return __uint_as_float(v & 0xffff0000u); }
DI u32 pkbf(float a, float b) {
  bf2 r;
  r[0] = (__bf16)a;
  r[1] = (__bf16)b;
  return __builtin_bit_cast(u32, r);
}
DI float rbf(float a) { return (float)(__bf16)a; }

DI f2 cmul(f2 a, f2 b) {
  f2 t = (f2){a.x, a.x} * b;
  f2 s = (f2){-b.y, b.x};
  return __builtin_elementwise_fma((f2){a.y, a.y}, s, t);
}
DI f2 cmulc(f2 a, f2 b) {
  f2 t = (f2){a.x, a.x} * (f2){b.x, -b.y};
  f2 s = (f2){b.y, b.x};
  return __builtin_elementwise_fma((f2){a.y, a.y}, s, t);
}
template <int SGN>
DI f2 rot90(f2 a) {
  return SGN > 0 ? (f2){a.y, -a.x} : (f2){-a.y, a.x};
}
DI f2 twid(int num, float invden) {
  float p = (float)num * invden;
  return (f2){__builtin_amdgcn_cosf(p), -__builtin_amdgcn_sinf(p)};
}

template <int SGN>
DI void dft8(f2 (&v)[8]) {
  const float R = 0.70710678118654752f;
  f2 b0 = v[0] + v[4], b4 = v[0] - v[4];
  f2 b1 = v[1] + v[5], b5 = v[1] - v[5];
  f2 b2 = v[2] + v[6], b6 = v[2] - v[6];
  f2 b3 = v[3] + v[7], b7 = v[3] - v[7];
  f2 d0 = b0 + b2, d1 = b0 - b2, d2 = b1 + b3, d3 = rot90<SGN>(b1 - b3);
  v[0] = d0 + d2;
  v[4] = d0 - d2;
  v[2] = d1 + d3;
  v[6] = d1 - d3;
  f2 c2 = rot90<SGN>(b6);
  f2 e0 = b4 + c2, e1 = b4 - c2;
  f2 p = b5 + b7, m = b5 - b7;
  f2 e2 = m + rot90<SGN>(p);
  f2 e3 = rot90<SGN>(p) - m;
  f2 RR = (f2){R, R};
  v[1] = __builtin_elementwise_fma(e2, RR, e0);
  v[5] = __builtin_elementwise_fma(e2, -RR, e0);
  v[3] = __builtin_elementwise_fma(e3, RR, e1);
  v[7] = __builtin_elementwise_fma(e3, -RR, e1);
}

DI void lds_wave_sync() { asm volatile("s_waitcnt lgkmcnt(0)" ::: "memory"); }

DI void fft512_fwd(f2 (&v)[8], const f2 (&tw1)[7], const f2 (&tw2)[7], LAS f2* buf, int L, int lane) {
  dft8<1>(v);
#pragma unroll
  for (int r = 1; r < 8; ++r) v[r] = cmul(v[r], tw1[r - 1]);
#pragma unroll
  for (int r = 0; r < 8; ++r) buf[72 * r + L] = v[r];
  lds_wave_sync();
  {
    const int a = 72 * (lane >> 3) + (lane & 7);
#pragma unroll
    for (int r = 0; r < 8; ++r) v[r] = buf[a + 8 * r];
  }
  dft8<1>(v);
#pragma unroll
  for (int r = 1; r < 8; ++r) v[r] = cmul(v[r], tw2[r - 1]);
  lds_wave_sync();
  {
    const int a = 66 * (lane & 7) + (lane >> 3);
#pragma unroll
    for (int r = 0; r < 8; ++r) buf[a + 8 * r] = v[r];
  }
  lds_wave_sync();
#pragma unroll
  for (int r = 0; r < 8; ++r) v[r] = buf[66 * r + lane];
  dft8<1>(v);
}

DI void fft512_inv(f2 (&v)[8], const f2 (&tw1)[7], const f2 (&tw2)[7], LAS f2* buf, int lane) {
  dft8<-1>(v);
  lds_wave_sync();
#pragma unroll
  for (int r = 0; r < 8; ++r) buf[68 * r + lane] = v[r];
  lds_wave_sync();
  {
    const int a = 68 * (lane & 7) + (lane >> 3);
#pragma unroll
    for (int r = 0; r < 8; ++r) v[r] = buf[a + 8 * r];
  }
#pragma unroll
  for (int r = 1; r < 8; ++r) v[r] = cmulc(v[r], tw2[r - 1]);
  dft8<-1>(v);
  lds_wave_sync();
  {
    const int a = 72 * (lane >> 3) + (lane & 7);
#pragma unroll
    for (int r = 0; r < 8; ++r) buf[a + 8 * r] = v[r];
  }
  lds_wave_sync();
#pragma unroll
  for (int r = 0; r < 8; ++r) v[r] = buf[72 * r + lane];
#pragma unroll
  for (int r = 1; r < 8; ++r) v[r] = cmulc(v[r], tw1[r - 1]);
  dft8<-1>(v);
}

template <int CTRL>
DI float dppf(float v) {
  return __builtin_bit_cast(float, __builtin_amdgcn_update_dpp(0, __builtin_bit_cast(int, v), CTRL, 0xf, 0xf, true));
}
DI float half_sum(float v) {
  v += dppf<0xB1>(v);
  v += dppf<0x4E>(v);
  v += dppf<0x141>(v);
  v += dppf<0x140>(v);
  u32 b = __float_as_uint(v);
  auto r = __builtin_amdgcn_permlane16_swap(b, b, false, false);
  return __uint_as_float(r[0]) + __uint_as_float(r[1]);
}

constexpr int WS_ = 584;

__global__ __launch_bounds__(256) void k1_rows(const u32* __restrict__ x32, const u16* __restrict__ wr,
                                               const u16* __restrict__ wi, const u32* __restrict__ gw32,
                                               char* __restrict__ ws) {
  __shared__ __attribute__((aligned(16))) char smem[4 * WS_ * 8 + 8192];
  LAS f2* zb = (LAS f2*)smem;
  LAS u32* hb = (LAS u32*)(smem + 4 * WS_ * 8);
  const int tid = threadIdx.x, lane = tid & 63, w = tid >> 6;
  const int b = blockIdx.x;
  const int grp = (b & 7) * 32 + (b >> 3);
  const int s0 = grp * 8;
  const int half = lane >> 5, l5 = lane & 31;
  const int row = s0 + 2 * w + half;

  u32 xv[8], gv[8];
#pragma unroll
  for (int j = 0; j < 8; ++j) xv[j] = x32[row * 256 + 32 * j + l5];
#pragma unroll
  for (int j = 0; j < 8; ++j) gv[j] = gw32[32 * j + l5];

  u32 wv[8];
  const int cg = grp & 3, pg = grp >> 2;
  {
    const int p0 = 32 * pg + 8 * w;
    const int q = p0 >> 9, lam = (p0 & 511) >> 3;
    const int col = 64 * cg + lane;
#pragma unroll
    for (int r = 0; r < 8; ++r) {
      const int k1 = q + 4 * (lam + 64 * r);
      wv[r] = (u32)wr[k1 * DF_ + col] | ((u32)wi[k1 * DF_ + col] << 16);
    }
  }
  u32 wn = 0;
  if (cg == 0 && tid < 32) {
    const int p = 32 * pg + tid;
    const int k1 = (p >> 9) + 4 * (((p & 511) >> 3) + 64 * (p & 7));
    wn = (u32)wr[k1 * DF_ + 256] | ((u32)wi[k1 * DF_ + 256] << 16);
  }

  const int L = 2 * l5 + half;
  f2 tw1[7], tw2[7];
#pragma unroll
  for (int r = 1; r < 8; ++r) {
    tw1[r - 1] = twid((L * r) & 511, 1.f / 512.f);
    tw2[r - 1] = twid(((lane & 7) * r) & 63, 1.f / 64.f);
  }

  if (w < 2) {
    const int fi = 2 * grp + w, ks = fi >> 4, db = fi & 15;
    const int d = 32 * db + l5;
    float gval[8];
#pragma unroll
    for (int j = 0; j < 8; ++j) {
      const int kp = 16 * ks + 8 * (j >> 2) + 4 * half + (j & 3);
      const int k2 = kp >> 1;
      const float ph = (float)((k2 * d) & 511) * (1.f / 512.f);
      float val = (j & 1) ? -2.f * __builtin_amdgcn_sinf(ph) : 2.f * __builtin_amdgcn_cosf(ph);
      if (kp == 0) val = 1.f;
      if (kp == 1) val = (d & 1) ? -1.f : 1.f;
      gval[j] = val;
    }
    u4 o = {pkbf(gval[0], gval[1]), pkbf(gval[2], gval[3]), pkbf(gval[4], gval[5]), pkbf(gval[6], gval[7])};
    *(u4*)(ws + OFF_UP + (size_t)(112 + ks) * 16384 + db * 1024 + lane * 16) = o;
  }

  float e0[8], e1[8];
  float sum = 0.f, dot = 0.f;
#pragma unroll
  for (int j = 0; j < 8; ++j) {
    e0[j] = bflo(xv[j]);
    e1[j] = bfhi(xv[j]);
    sum += e0[j] + e1[j];
    dot += rbf(e0[j] * bflo(gv[j])) + rbf(e1[j] * bfhi(gv[j]));
  }
  sum = half_sum(sum);
  dot = half_sum(dot);
  const float mean = sum * (1.f / 512.f);
  float sq = 0.f;
#pragma unroll
  for (int j = 0; j < 8; ++j) {
    const float a = e0[j] - mean, c = e1[j] - mean;
    sq += a * a + c * c;
  }
  sq = half_sum(sq);
  const float mb = rbf(mean);
  const float den = rbf(__builtin_amdgcn_sqrtf(rbf(rbf(sq * (1.f / 512.f)) + 1e-5f)));
  const float rinv = __builtin_amdgcn_rcpf(den);
  if (l5 == 0) {
    const float z = rbf(dot);
    const float gate = rbf(__builtin_amdgcn_rcpf(1.f + __expf(-z)));
    *(float*)(ws + OFF_G + row * 4) = gate;
  }

  f2 v[8];
#pragma unroll
  for (int j = 0; j < 8; ++j) {
    const float h0 = rbf(e0[j] - mb) * rinv, h1 = rbf(e1[j] - mb) * rinv;
    auto rh = __builtin_amdgcn_permlane32_swap(__float_as_uint(h0), __float_as_uint(h1), false, false);
    hb[(64 * j + L) * 4 + w] = pkbf(__uint_as_float(rh[0]), __uint_as_float(rh[1]));
    auto rx = __builtin_amdgcn_permlane32_swap(__float_as_uint(e0[j]), __float_as_uint(e1[j]), false, false);
    v[j] = (f2){__uint_as_float(rx[0]), __uint_as_float(rx[1])};
  }

  {
    u32* dst = (u32*)(ws + OFF_WT) + (size_t)(64 * cg + lane) * 2048 + 32 * pg + 8 * w;
    *(u4*)dst = (u4){wv[0], wv[1], wv[2], wv[3]};
    *(u4*)(dst + 4) = (u4){wv[4], wv[5], wv[6], wv[7]};
    if (cg == 0 && tid < 32) ((u32*)(ws + OFF_WT))[256 * 2048 + 32 * pg + tid] = wn;
  }

  LAS f2* buf = zb + w * WS_;
  fft512_fwd(v, tw1, tw2, buf, L, lane);
  lds_wave_sync();
#pragma unroll
  for (int r = 0; r < 8; ++r) buf[64 * r + lane] = v[r];
  __syncthreads();

  {
    char* hp = ws + OFF_HP + (size_t)(s0 >> 4) * 16384 + ((s0 >> 3) & 1) * 512;
#pragma unroll
    for (int i = 0; i < 2; ++i) {
      const int d = tid + 256 * i;
      u4 c = *(LAS u4*)(hb + d * 4);
      *(u4*)(hp + (d >> 5) * 1024 + (d & 31) * 16) = c;
    }
  }
  {
    const int pw = tid & 3, kk = tid >> 2;
    LAS f2* zw = zb + pw * WS_;
    f4* T = (f4*)(ws + OFF_T);
#pragma unroll
    for (int i = 0; i < 4; ++i) {
      const int k = kk + 64 * i;
      const f2 za = zw[k], zc = zw[(512 - k) & 511];
      T[(size_t)k * 1024 + (s0 >> 1) + pw] = (f4){za.x + zc.x, za.y - zc.y, za.y + zc.y, zc.x - za.x};
    }
    if (tid < 4) {
      const f2 za = zw[256];
      T[(size_t)256 * 1024 + (s0 >> 1) + pw] = (f4){2.f * za.x, 0.f, 2.f * za.y, 0.f};
    }
  }
}

constexpr int RING = 8;
constexpr int STAGE = 4096;
constexpr int WRING = RING * STAGE;

DI u4 mk_srd(const void* p, u32 bytes) {
  const uint64_t a = (uint64_t)p;
  u4 r;
  r[0] = __builtin_amdgcn_readfirstlane((u32)a);
  r[1] = __builtin_amdgcn_readfirstlane((u32)(a >> 32) & 0xffffu);
  r[2] = __builtin_amdgcn_readfirstlane(bytes);
  r[3] = 0x00020000u;
  return r;
}

DI void dma_stage(u32 m0v, u32 vP, u4 sP, u32 oP, u32 vR0, u32 vR1, u4 sR, u32 oR) {
  asm volatile(
      "s_mov_b32 m0, %0\n\ts_nop 0\n\t"
      "buffer_load_dwordx4 %1, %2, %3 offen lds\n\t"
      "buffer_load_dwordx4 %1, %2, %3 offen offset:1024 lds\n\t"
      "buffer_load_dwordx4 %4, %6, %7 offen offset:2048 lds\n\t"
      "buffer_load_dwordx4 %5, %6, %7 offen offset:3072 lds"
      :
      : "s"(m0v), "v"(vP), "s"(sP), "s"(oP), "v"(vR0), "v"(vR1), "s"(sR), "s"(oR)
      : "memory");
}

DI bf8 ld_frag(u32 a) { return *(LAS bf8*)(uintptr_t)a; }
DI bf8 ld_tr(u32 a, u32 toff) {
  bf4 lo = __builtin_amdgcn_ds_read_tr16_b64_v4bf16((LAS bf4*)(uintptr_t)a);
  bf4 hi = __builtin_amdgcn_ds_read_tr16_b64_v4bf16((LAS bf4*)(uintptr_t)(a + toff));
  return __builtin_shufflevector(lo, hi, 0, 1, 2, 3, 4, 5, 6, 7);
}

template <int KMAP, bool RA>
DI void gemm_wave(f16v (&acc)[4], u4 sP, u32 oP, u4 sR, u32 oR, u32 rstride, int nsteps, u32 ldsb, int lane) {
  const u32 vP = lane * 16;
  const u32 rr = lane >> 3, ch = (lane & 7) ^ (((rr >> 1) & 1) << 2);
  const u32 vR0 = rr * rstride + ch * 16 + 2048;
  const u32 vR1 = vR0 + 8 * rstride - 1024;
  const u32 stepR = 16 * rstride;
  const int hh = lane >> 5, blk = (lane >> 4) & 1, q = (lane & 15) >> 2, p = lane & 3;
  const u32 rowb = (KMAP == 0 ? 8 * hh : 4 * hh) + q;
  const u32 toff = KMAP == 0 ? 512 : 1024;
  const u32 rdP = ldsb + lane * 16;
  const u32 rdRa = ldsb + 2048 + rowb * 128 + (q >> 1) * 64 + (2 * blk + (p >> 1)) * 16 + 8 * (p & 1);
  const u32 rdRb = rdRa ^ 64;

  asm volatile("s_nop 4" ::: "memory");
#pragma unroll 1
  for (int i = 0; i < RING; ++i) {
    dma_stage(ldsb + i * STAGE, vP, sP, oP, vR0, vR1, sR, oR);
    oP += 16384;
    oR += stepR;
  }
  bf8 P0[2], R0[2], P1[2], R1[2];
  asm volatile("s_waitcnt vmcnt(%0)" ::"n"(4 * (RING - 1)) : "memory");
  P0[0] = ld_frag(rdP);
  P0[1] = ld_frag(rdP + 1024);
  R0[0] = ld_tr(rdRa, toff);
  R0[1] = ld_tr(rdRb, toff);
  u32 so = 0;
#pragma unroll 1
  for (int i = 0; i < nsteps; i += 2) {
    u32 sn = (so + STAGE) & (WRING - 1);
    asm volatile("s_waitcnt vmcnt(%0) lgkmcnt(0)" ::"n"(4 * (RING - 2)) : "memory");
    dma_stage(ldsb + so, vP, sP, oP, vR0, vR1, sR, oR);
    oP += 16384;
    oR += stepR;
    P1[0] = ld_frag(rdP + sn);
    P1[1] = ld_frag(rdP + sn + 1024);
    R1[0] = ld_tr(rdRa + sn, toff);
    R1[1] = ld_tr(rdRb + sn, toff);
#pragma unroll
    for (int m = 0; m < 2; ++m)
#pragma unroll
      for (int n = 0; n < 2; ++n)
        acc[2 * m + n] = RA ? __builtin_amdgcn_mfma_f32_32x32x16_bf16(R0[m], P0[n], acc[2 * m + n], 0, 0, 0)
                            : __builtin_amdgcn_mfma_f32_32x32x16_bf16(P0[m], R0[n], acc[2 * m + n], 0, 0, 0);
    so = (sn + STAGE) & (WRING - 1);
    asm volatile("s_waitcnt vmcnt(%0) lgkmcnt(0)" ::"n"(4 * (RING - 2)) : "memory");
    dma_stage(ldsb + sn, vP, sP, oP, vR0, vR1, sR, oR);
    oP += 16384;
    oR += stepR;
    P0[0] = ld_frag(rdP + so);
    P0[1] = ld_frag(rdP + so + 1024);
    R0[0] = ld_tr(rdRa + so, toff);
    R0[1] = ld_tr(rdRb + so, toff);
#pragma unroll
    for (int m = 0; m < 2; ++m)
#pragma unroll
      for (int n = 0; n < 2; ++n)
        acc[2 * m + n] = RA ? __builtin_amdgcn_mfma_f32_32x32x16_bf16(R1[m], P1[n], acc[2 * m + n], 0, 0, 0)
                            : __builtin_amdgcn_mfma_f32_32x32x16_bf16(P1[m], R1[n], acc[2 * m + n], 0, 0, 0);
  }
  asm volatile("s_waitcnt vmcnt(0) lgkmcnt(0)" ::: "memory");
}

DI float gelu_tanh(float x) {
  const float y = 0.7978845608028654f * (x + 0.044715f * x * x * x);
  const float e = __builtin_amdgcn_exp2f(-2.885390081777927f * y);
  return x * __builtin_amdgcn_rcpf(1.f + e);
}

constexpr int K2_LDS = 4 * WRING + 4 * WS_ * 8;

DI void col_fft(const char* ws, int col, int q, int lane, LAS f2* zb, u16* Yt) {
  const f2* Tc = (const f2*)(ws + OFF_T) + (size_t)col * 2048;
  f2 a[4][8];
#pragma unroll
  for (int r = 0; r < 8; ++r)
#pragma unroll
    for (int j = 0; j < 4; ++j) a[j][r] = Tc[512 * j + 64 * r + lane];
  const u4* Wp = (const u4*)(ws + OFF_WT + ((size_t)col * 2048 + q * 512 + lane * 8) * 4);
  const u4 wa = Wp[0], wb = Wp[1];
  f2 tw1[7], tw2[7], tq[8];
#pragma unroll
  for (int r = 1; r < 8; ++r) {
    tw1[r - 1] = twid((lane * r) & 511, 1.f / 512.f);
    tw2[r - 1] = twid(((lane & 7) * r) & 63, 1.f / 64.f);
  }
#pragma unroll
  for (int r = 0; r < 8; ++r) tq[r] = twid(((64 * r + lane) * q) & 2047, 1.f / 2048.f);
  const float s2 = (q & 1) ? -1.f : 1.f;
  const f2 cq = (f2){(q & 1) ? 0.f : (q ? -1.f : 1.f), (q & 1) ? (q == 1 ? -1.f : 1.f) : 0.f};
  f2 v[8];
#pragma unroll
  for (int r = 0; r < 8; ++r) {
    const f2 e = __builtin_elementwise_fma(a[2][r], (f2){s2, s2}, a[0][r]);
    const f2 o = __builtin_elementwise_fma(a[3][r], (f2){s2, s2}, a[1][r]);
    v[r] = cmul(e + cmul(o, cq), tq[r]);
  }
  LAS f2* buf = zb + q * WS_;
  fft512_fwd(v, tw1, tw2, buf, lane, lane);
#pragma unroll
  for (int r = 0; r < 8; ++r) {
    const u32 ww = r < 4 ? wa[r & 3] : wb[r & 3];
    v[r] = cmul(v[r], (f2){bflo(ww), bfhi(ww)});
  }
  fft512_inv(v, tw1, tw2, buf, lane);
  lds_wave_sync();
#pragma unroll
  for (int r = 0; r < 8; ++r) buf[64 * r + lane] = cmulc(v[r], tq[r]);
  lds_wave_sync();
  __builtin_amdgcn_s_barrier();
  asm volatile("" ::: "memory");
  const int t = q * 64 + lane;
  f4 z[4];
#pragma unroll
  for (int i = 0; i < 4; ++i) z[i] = *(LAS f4*)(zb + i * WS_ + 2 * t);
  const f4 p02 = z[0] + z[2], m02 = z[0] - z[2], p13 = z[1] + z[3], m13 = z[1] - z[3];
  const f4 im13 = (f4){-m13.y, m13.x, -m13.w, m13.z};
  f4 y[4] = {p02 + p13, m02 + im13, p02 - p13, m02 - im13};
  const int rre = col == 256 ? 1 : 2 * col;
  u32* yr = (u32*)(Yt + (size_t)rre * 2048) + t;
  u32* yi = yr + 1024;
#pragma unroll
  for (int j = 0; j < 4; ++j) yr[256 * j] = pkbf(y[j].x, y[j].z);
  if (col != 0 && col != 256) {
#pragma unroll
    for (int j = 0; j < 4; ++j) yi[256 * j] = pkbf(y[j].y, y[j].w);
  }
}

__global__ __launch_bounds__(512) void k2_mid(const u16* __restrict__ w1, const u16* __restrict__ b1,
                                              char* __restrict__ ws) {
  extern __shared__ __attribute__((aligned(16))) char smem[];
  const int tid = threadIdx.x, lane = tid & 63;
  const int w = __builtin_amdgcn_readfirstlane(tid >> 6);
  const int b = blockIdx.x, xg = b & 7, i = b >> 3;
  const u32 lds0 = (u32)(uintptr_t)(LAS char*)smem;
  u16* Yt = (u16*)(ws + OFF_YT);
  if (w >= 4) {
    col_fft(ws, b, w - 4, lane, (LAS f2*)(uintptr_t)(lds0 + 4 * WRING), Yt);
    return;
  }
  if (i >= 28) {
    if (b == 255) col_fft(ws, 256, w, lane, (LAS f2*)(uintptr_t)lds0, Yt);
    return;
  }
  const int dt = 4 * (xg & 1) + (i & 3), ft = 7 * (xg >> 1) + (i >> 2);
  f16v acc[4];
#pragma unroll
  for (int t = 0; t < 4; ++t)
#pragma unroll
    for (int r = 0; r < 16; ++r) acc[t][r] = 0.f;
  const int ks0 = 32 * w;
  const u32 ldsb = lds0 + w * WRING;
  const u4 sP = mk_srd(ws + OFF_HP, (u32)(ks0 + 32) * 16384u);
  const u4 sR = mk_srd((const char*)w1 - 4096, (u32)(ks0 + 32) * 16u * (F_ * 2) + 4096u);
  gemm_wave<0, true>(acc, sP, ks0 * 16384 + dt * 2048, sR, ks0 * 16 * (F_ * 2) + ft * 128, F_ * 2, 32, ldsb, lane);
#pragma unroll
  for (int t = 0; t < 4; ++t)
#pragma unroll
    for (int g = 0; g < 4; ++g)
      *(LAS f4*)(uintptr_t)(ldsb + t * 4096 + g * 1024 + lane * 16) =
          (f4){acc[t][4 * g], acc[t][4 * g + 1], acc[t][4 * g + 2], acc[t][4 * g + 3]};
  const int mb = w >> 1, nb = w & 1, hh = lane >> 5;
  u2 bias[4];
#pragma unroll
  for (int g = 0; g < 4; ++g) bias[g] = *(const u2*)(b1 + ft * 64 + mb * 32 + 8 * g + 4 * hh);
  lds_wave_sync();
  __builtin_amdgcn_s_barrier();
  asm volatile("" ::: "memory");
  u32 o[8];
#pragma unroll
  for (int g = 0; g < 4; ++g) {
    const u32 a = lds0 + w * 4096 + g * 1024 + lane * 16;
    const f4 s = (*(LAS f4*)(uintptr_t)(a) + *(LAS f4*)(uintptr_t)(a + WRING)) +
                 (*(LAS f4*)(uintptr_t)(a + 2 * WRING) + *(LAS f4*)(uintptr_t)(a + 3 * WRING));
    const float u0 = gelu_tanh(rbf(rbf(s.x) + bflo(bias[g].x)));
    const float u1 = gelu_tanh(rbf(rbf(s.y) + bfhi(bias[g].x)));
    const float u2_ = gelu_tanh(rbf(rbf(s.z) + bflo(bias[g].y)));
    const float u3 = gelu_tanh(rbf(rbf(s.w) + bfhi(bias[g].y)));
    o[2 * g] = pkbf(u0, u1);
    o[2 * g + 1] = pkbf(u2_, u3);
  }
  char* up = ws + OFF_UP + (size_t)(ft * 4 + mb * 2) * 16384 + (dt * 2 + nb) * 1024 + lane * 16;
  *(u4*)up = (u4){o[0], o[1], o[2], o[3]};
  *(u4*)(up + 16384) = (u4){o[4], o[5], o[6], o[7]};
}

constexpr int K3_LDS = 4 * WRING;

__global__ __launch_bounds__(256) void k3_out(const u16* __restrict__ w2, const u16* __restrict__ b2,
                                              const char* __restrict__ ws, u16* __restrict__ out) {
  extern __shared__ __attribute__((aligned(16))) char smem[];
  const int tid = threadIdx.x, lane = tid & 63;
  const int w = __builtin_amdgcn_readfirstlane(tid >> 6);
  const int b = blockIdx.x, xg = b & 7, i = b >> 3;
  const u32 lds0 = (u32)(uintptr_t)(LAS char*)smem;
  const int dt = 4 * (xg & 1) + (i & 3), st = 8 * (xg >> 1) + (i >> 2);
  f16v acc[4];
#pragma unroll
  for (int t = 0; t < 4; ++t)
#pragma unroll
    for (int r = 0; r < 16; ++r) acc[t][r] = 0.f;
  const int ks0 = w < 3 ? 38 * w : 112;
  const int nst = w < 2 ? 38 : (w == 2 ? 36 : 32);
  const int kr0 = w < 3 ? ks0 : 0;
  const char* rbase = w < 3 ? (const char*)w2 : ws + OFF_YT;
  const u32 ldsb = lds0 + w * WRING;
  const u4 sP = mk_srd(ws + OFF_UP, (u32)(ks0 + nst) * 16384u);
  const u4 sR = mk_srd(rbase - 4096, (u32)(kr0 + nst) * 65536u + 4096u);
  gemm_wave<1, false>(acc, sP, ks0 * 16384 + dt * 2048, sR, kr0 * 65536 + st * 128, 4096, nst, ldsb, lane);
  const int hh = lane >> 5;
#pragma unroll
  for (int m = 0; m < 2; ++m)
#pragma unroll
    for (int n = 0; n < 2; ++n)
#pragma unroll
      for (int g = 0; g < 4; ++g)
        *(LAS f4*)(uintptr_t)(ldsb + (n * 32 + (lane & 31)) * 272 + (m * 32 + 8 * g + 4 * hh) * 4) =
            (f4){acc[2 * m + n][4 * g], acc[2 * m + n][4 * g + 1], acc[2 * m + n][4 * g + 2],
                 acc[2 * m + n][4 * g + 3]};
  float gs[2], bs[2];
#pragma unroll
  for (int h = 0; h < 2; ++h) {
    const int s = st * 64 + (tid >> 3) + 32 * h;
    gs[h] = *(const float*)(ws + OFF_G + s * 4);
    bs[h] = bflo(b2[s]);
  }
  lds_wave_sync();
  __builtin_amdgcn_s_barrier();
  asm volatile("" ::: "memory");
#pragma unroll
  for (int h = 0; h < 2; ++h) {
    const int r = (tid >> 3) + 32 * h;
    const float g = gs[h], og = rbf(1.f - g);
    u32 o[4];
#pragma unroll
    for (int c = 0; c < 2; ++c) {
      const u32 a = lds0 + r * 272 + (tid & 7) * 32 + c * 16;
      const f4 sb = *(LAS f4*)(uintptr_t)(a) + *(LAS f4*)(uintptr_t)(a + WRING) + *(LAS f4*)(uintptr_t)(a + 2 * WRING);
      const f4 sa = *(LAS f4*)(uintptr_t)(a + 3 * WRING);
      float res[4];
#pragma unroll
      for (int e = 0; e < 4; ++e) {
        const float av = rbf(sa[e] * (1.f / 2097152.f));
        const float bv = rbf(rbf(sb[e]) + bs[h]);
        res[e] = rbf(g * av) + rbf(og * bv);
      }
      o[2 * c] = pkbf(res[0], res[1]);
      o[2 * c + 1] = pkbf(res[2], res[3]);
    }
    *(u4*)(out + (size_t)(st * 64 + r) * D_ + dt * 64 + (tid & 7) * 8) = (u4){o[0], o[1], o[2], o[3]};
  }
}

extern "C" void kernel_launch(void* const* d_in, const int* in_sizes, int n_in, void* d_out, int out_size,
                              void* d_ws, size_t ws_size, hipStream_t stream) {
  const u32* x = (const u32*)d_in[0];
  const u16* wr = (const u16*)d_in[1];
  const u16* wi = (const u16*)d_in[2];
  const u16* w1 = (const u16*)d_in[4];
  const u16* b1 = (const u16*)d_in[5];
  const u16* w2 = (const u16*)d_in[6];
  const u16* b2 = (const u16*)d_in[7];
  const u32* gw = (const u32*)d_in[8];
  char* ws = (char*)d_ws;
  (void)hipFuncSetAttribute((const void*)k2_mid, hipFuncAttributeMaxDynamicSharedMemorySize, K2_LDS);
  (void)hipFuncSetAttribute((const void*)k3_out, hipFuncAttributeMaxDynamicSharedMemorySize, K3_LDS);
  k1_rows<<<256, 256, 0, stream>>>(x, wr, wi, gw, ws);
  k2_mid<<<256, 512, K2_LDS, stream>>>(w1, b1, ws);
  k3_out<<<256, 256, K3_LDS, stream>>>(w2, b2, ws, (u16*)d_out);
}
```

```cpp
#include <hip/hip_runtime.h>
#include <stdint.h>
#include <type_traits>

typedef unsigned short u16;
typedef unsigned int u32;
typedef __attribute__((ext_vector_type(2))) float f2;
typedef __attribute__((ext_vector_type(4))) float f4;
typedef __attribute__((ext_vector_type(16))) float f16v;
typedef __attribute__((ext_vector_type(2))) __bf16 bf2;
typedef __attribute__((ext_vector_type(4))) __bf16 bf4;
typedef __attribute__((ext_vector_type(8))) __bf16 bf8;
typedef __attribute__((ext_vector_type(2))) u32 u2;
typedef __attribute__((ext_vector_type(4))) u32 u4;
#define LAS __attribute__((address_space(3)))
#define DI __device__ __forceinline__
#define SC1 16
#define SB() __builtin_amdgcn_sched_barrier(0)

constexpr int S_ = 2048, D_ = 512, F_ = 1792, DF_ = 257;

constexpr u32 OFF_HP = 0;
constexpr u32 OFF_T = OFF_HP + 2097152;
constexpr u32 OFF_TN = OFF_T + 64 * 32768;
constexpr u32 OFF_WT = OFF_TN + 8192;
constexpr u32 OFF_G = OFF_WT + 257 * 2048 * 4;
constexpr u32 OFF_UP = OFF_G + 8192;
constexpr u32 OFF_YT = OFF_UP + 112 * 16384;
constexpr u32 OFF_FA = OFF_YT + 2097152;
constexpr u32 OFF_FB = OFF_FA + 65536;
constexpr u32 OFF_GO = OFF_FB + 65536;
constexpr u32 OFF_E = OFF_GO + 2048;
constexpr u32 WS_BYTES = OFF_E + 256;

constexpr u32 WRING = 32768;
constexpr int WS_ = 584;
constexpr u32 L_HT = 3 * WRING + 12 * 2048;
constexpr u32 L_XS = 2 * WRING + 12 * 2048;
constexpr u32 L_GW = 1 * WRING + 15 * 2048;
constexpr u32 L_EX = 4 * WRING;
constexpr u32 L_WT = L_EX + 4 * WS_ * 8;
constexpr u32 L_JUNK = L_WT + 64 * 144 + 128;
constexpr u32 L_CT = L_JUNK + 1024;
constexpr u32 L_B1 = L_CT + 256;
constexpr u32 L_GB = L_B1 + 128;
constexpr u32 L_CF = L_GB + 32;
constexpr u32 LDS_BYTES = L_CF + 768;
enum { C_X2, C_X3, C_X4, C_G3, C_G4, C_PA, C_PB, C_TL, C_G0, C_G1, C_G2, F_A, F_B, C_PRE, C_XR, W_TAG, W_JUNK, F_XS, F_FREE = F_XS + 4 };

#define POLL_SLEEP 3
#define WATCH_SLEEP 0
#define GPRIO1 0
#define GPRIO2 0
#define XSL false
#define YDELAY 0
#define ROTS1 0
#define ROTS2 0
#define CPK false
#define XPRIO_B 2
#define SPLITW 1
#define REPS 8
#define GO1 F_A
#define GO1N 1
#define PDV 5
#define ARR1 0
#define ARR2 0
#define NRV 16
constexpr int NR = NRV;
constexpr int PD = PDV, RS = PD + 1;
constexpr int W2AT = 31 - PD > 26 ? 31 - PD : 26;
constexpr int SPIN_G = 200000;
constexpr int SPIN_L = 4000000;

#define STAMP_DECL
#define CLK(x)
#define CK_(i)
#define ST_ARG
#define ST_PASS
#define STAMP(i)
#define STAMP_FLUSH()

template <int I, int N, class Fn>
DI void sfor(Fn&& f) {
  if constexpr (I < N) {
    f(std::integral_constant<int, I>{});
    sfor<I + 1, N>(f);
  }
}

DI float bflo(u32 v) { return __uint_as_float(v << 16); }
DI float bfhi(u32 v) { return __uint_as_float(v & 0xffff0000u); }
DI u32 pkbf(float a, float b) {
  bf2 r;
  r[0] = (__bf16)a;
  r[1] = (__bf16)b;
  return __builtin_bit_cast(u32, r);
}
DI float rbf(float a) { return (float)(__bf16)a; }

DI f2 twid(int num, float invden) {
  float p = (float)num * invden;
  return (f2){__builtin_amdgcn_cosf(p), -__builtin_amdgcn_sinf(p)};
}

#define PK_SUB " neg_lo:[0,1] neg_hi:[0,1]\n\t"
#define ROT(d, a, b, NEG) "v_pk_fma_f32 %" #d ", %" #b ", 1.0, %" #a " op_sel:[1,0,0] op_sel_hi:[0,0,1] " NEG "\n\t"
#define NG_RM "neg_hi:[1,0,0]"
#define NG_RP "neg_lo:[1,0,0]"
#define DFT8_BODY(RA, RB)                                                        \
  "v_pk_add_f32 %8, %0, %4\n\tv_pk_add_f32 %12, %0, %4" PK_SUB                   \
  "v_pk_add_f32 %10, %2, %6\n\tv_pk_add_f32 %14, %2, %6" PK_SUB                  \
  "v_pk_add_f32 %9, %1, %5\n\tv_pk_add_f32 %13, %1, %5" PK_SUB                   \
  "v_pk_add_f32 %11, %3, %7\n\tv_pk_add_f32 %15, %3, %7" PK_SUB                  \
  "v_pk_add_f32 %0, %8, %10\n\tv_pk_add_f32 %2, %8, %10" PK_SUB                  \
  ROT(1, 12, 14, RA) ROT(3, 12, 14, RB)                                          \
  "v_pk_add_f32 %5, %13, %15\n\tv_pk_add_f32 %7, %13, %15" PK_SUB                \
  "v_pk_add_f32 %4, %9, %11\n\tv_pk_add_f32 %6, %9, %11" PK_SUB                  \
  ROT(9, 7, 5, RA) ROT(11, 7, 5, RB)                                             \
  "v_pk_add_f32 %8, %0, %4\n\tv_pk_add_f32 %12, %0, %4" PK_SUB                   \
  ROT(10, 2, 6, RA) ROT(14, 2, 6, RB)                                            \
  "v_pk_fma_f32 %13, %9, %16, %1 op_sel_hi:[1,0,1] neg_lo:[0,1,0] neg_hi:[0,1,0]\n\t" \
  "v_pk_fma_f32 %15, %11, %16, %3 op_sel_hi:[1,0,1]\n\t"                         \
  "v_pk_fma_f32 %9, %9, %16, %1 op_sel_hi:[1,0,1]\n\t"                           \
  "v_pk_fma_f32 %11, %11, %16, %3 op_sel_hi:[1,0,1] neg_lo:[0,1,0] neg_hi:[0,1,0]"
template <int SGN>
DI void dft8(f2 (&v)[8]) {
  const f2 RR = {0.70710678118654752f, 0.70710678118654752f};
  f2 y[8];
  if constexpr (SGN > 0)
    asm(DFT8_BODY(NG_RM, NG_RP)
        : "+v"(v[0]), "+v"(v[1]), "+v"(v[2]), "+v"(v[3]), "+v"(v[4]), "+v"(v[5]), "+v"(v[6]), "+v"(v[7]), "=&v"(y[0]),
          "=&v"(y[1]), "=&v"(y[2]), "=&v"(y[3]), "=&v"(y[4]), "=&v"(y[5]), "=&v"(y[6]), "=&v"(y[7])
        : "s"(RR));
  else
    asm(DFT8_BODY(NG_RP, NG_RM)
        : "+v"(v[0]), "+v"(v[1]), "+v"(v[2]), "+v"(v[3]), "+v"(v[4]), "+v"(v[5]), "+v"(v[6]), "+v"(v[7]), "=&v"(y[0]),
          "=&v"(y[1]), "=&v"(y[2]), "=&v"(y[3]), "=&v"(y[4]), "=&v"(y[5]), "=&v"(y[6]), "=&v"(y[7])
        : "s"(RR));
#pragma unroll
  for (int r = 0; r < 8; ++r) v[r] = y[r];
}
#define CM_MUL " op_sel:[0,0] op_sel_hi:[0,1]\n\t"
#define CM_FMA " op_sel:[1,1,0] op_sel_hi:[1,0,1] neg_lo:[0,1,0]\n\t"
#define CC_MUL " op_sel:[0,0] op_sel_hi:[0,1] neg_hi:[0,1]\n\t"
#define CC_FMA " op_sel:[1,1,0] op_sel_hi:[1,0,1]\n\t"
#define CMUL4_BODY(M, A)                                                                     \
  "v_pk_mul_f32 %0, %4, %8" M "v_pk_mul_f32 %1, %5, %9" M "v_pk_mul_f32 %2, %6, %10" M       \
  "v_pk_mul_f32 %3, %7, %11" M "v_pk_fma_f32 %0, %4, %8, %0" A "v_pk_fma_f32 %1, %5, %9, %1" A \
  "v_pk_fma_f32 %2, %6, %10, %2" A "v_pk_fma_f32 %3, %7, %11, %3" A
#define CMUL3_BODY(M, A)                                                               \
  "v_pk_mul_f32 %0, %3, %6" M "v_pk_mul_f32 %1, %4, %7" M "v_pk_mul_f32 %2, %5, %8" M  \
  "v_pk_fma_f32 %0, %3, %6, %0" A "v_pk_fma_f32 %1, %4, %7, %1" A "v_pk_fma_f32 %2, %5, %8, %2" A
template <bool CONJ>
DI void cmul4(f2& a, f2& b, f2& c, f2& d, f2 wa, f2 wb, f2 wc, f2 wd) {
  f2 o0, o1, o2, o3;
  if constexpr (CONJ)
    asm(CMUL4_BODY(CC_MUL, CC_FMA) : "=&v"(o0), "=&v"(o1), "=&v"(o2), "=&v"(o3)
        : "v"(a), "v"(b), "v"(c), "v"(d), "v"(wa), "v"(wb), "v"(wc), "v"(wd));
  else
    asm(CMUL4_BODY(CM_MUL, CM_FMA) : "=&v"(o0), "=&v"(o1), "=&v"(o2), "=&v"(o3)
        : "v"(a), "v"(b), "v"(c), "v"(d), "v"(wa), "v"(wb), "v"(wc), "v"(wd));
  a = o0; b = o1; c = o2; d = o3;
}
template <bool CONJ>
DI void cmul3(f2& a, f2& b, f2& c, f2 wa, f2 wb, f2 wc) {
  f2 o0, o1, o2;
  if constexpr (CONJ)
    asm(CMUL3_BODY(CC_MUL, CC_FMA) : "=&v"(o0), "=&v"(o1), "=&v"(o2) : "v"(a), "v"(b), "v"(c), "v"(wa), "v"(wb), "v"(wc));
  else
    asm(CMUL3_BODY(CM_MUL, CM_FMA) : "=&v"(o0), "=&v"(o1), "=&v"(o2) : "v"(a), "v"(b), "v"(c), "v"(wa), "v"(wb), "v"(wc));
  a = o0; b = o1; c = o2;
}
template <bool CONJ>
DI void twid7(f2 (&v)[8], const f2 (&t)[7]) {
  cmul4<CONJ>(v[1], v[2], v[3], v[4], t[0], t[1], t[2], t[3]);
  cmul3<CONJ>(v[5], v[6], v[7], t[4], t[5], t[6]);
}

#define AS(s, d, a, b) "v_add_f32 %" #s ", %" #a ", %" #b "\n\tv_sub_f32 %" #d ", %" #a ", %" #b "\n\t"
struct SD { float s, d; };
DI void as4(SD& p, SD& q, SD& r, SD& t, float a0, float b0, float a1, float b1, float a2, float b2, float a3, float b3) {
  asm(AS(0, 1, 8, 9) AS(2, 3, 10, 11) AS(4, 5, 12, 13) AS(6, 7, 14, 15)
      : "=&v"(p.s), "=&v"(p.d), "=&v"(q.s), "=&v"(q.d), "=&v"(r.s), "=&v"(r.d), "=&v"(t.s), "=&v"(t.d)
      : "v"(a0), "v"(b0), "v"(a1), "v"(b1), "v"(a2), "v"(b2), "v"(a3), "v"(b3));
}
DI void as2(SD& p, SD& q, float a0, float b0, float a1, float b1) {
  asm(AS(0, 1, 4, 5) AS(2, 3, 6, 7) : "=&v"(p.s), "=&v"(p.d), "=&v"(q.s), "=&v"(q.d) : "v"(a0), "v"(b0), "v"(a1), "v"(b1));
}
#define FS(s, d, a, b) "v_fma_f32 %" #s ", %" #b ", %16, %" #a "\n\tv_fma_f32 %" #d ", -%" #b ", %16, %" #a "\n\t"
template <int SGN>
DI void dft8_s(f2 (&v)[8]) {
  SD r0, r1, r2, r3, i0, i1, i2, i3;
  as4(r0, r2, i0, i2, v[0].x, v[4].x, v[2].x, v[6].x, v[0].y, v[4].y, v[2].y, v[6].y);
  as4(r1, r3, i1, i3, v[1].x, v[5].x, v[3].x, v[7].x, v[1].y, v[5].y, v[3].y, v[7].y);
  SD dr, di, er, ei, fr, fi, pr, pi;
  as4(dr, di, er, ei, r0.s, r2.s, i0.s, i2.s, r0.d, i2.d, i0.d, r2.d);
  as4(pr, pi, fr, fi, r1.d, r3.d, i1.d, i3.d, r1.s, r3.s, i1.s, i3.s);
  SD gr, gi, y0r, y0i, y2r, y2i;
  as2(gr, gi, pr.d, pi.s, pi.d, pr.s);
  as4(y0r, y0i, y2r, y2i, dr.s, fr.s, di.s, fi.s, dr.d, fi.d, di.d, fr.d);
  const float e0r = SGN > 0 ? er.s : er.d, e0i = SGN > 0 ? ei.d : ei.s, e1r = SGN > 0 ? er.d : er.s, e1i = SGN > 0 ? ei.s : ei.d;
  const float e2r = SGN > 0 ? gr.s : gr.d, e2i = SGN > 0 ? gi.d : gi.s, e3r = SGN > 0 ? gr.d : gr.s, e3i = SGN > 0 ? gi.s : gi.d;
  SD a, b, c, d;
  asm(FS(0, 1, 8, 9) FS(2, 3, 10, 11) FS(4, 5, 12, 13) FS(6, 7, 14, 15)
      : "=&v"(a.s), "=&v"(a.d), "=&v"(b.s), "=&v"(b.d), "=&v"(c.s), "=&v"(c.d), "=&v"(d.s), "=&v"(d.d)
      : "v"(e0r), "v"(e2r), "v"(e0i), "v"(e2i), "v"(e1r), "v"(e3r), "v"(e1i), "v"(e3i), "s"(0.70710678118654752f));
  v[0] = (f2){y0r.s, y0i.s};
  v[4] = (f2){y0r.d, y0i.d};
  v[2] = SGN > 0 ? (f2){y2r.s, y2i.d} : (f2){y2r.d, y2i.s};
  v[6] = SGN > 0 ? (f2){y2r.d, y2i.s} : (f2){y2r.s, y2i.d};
  v[1] = (f2){a.s, b.s};
  v[5] = (f2){a.d, b.d};
  v[7] = (f2){c.s, d.s};
  v[3] = (f2){c.d, d.d};
}
#define SM(ox, oy, ax, wx, wy, N) "v_mul_f32 %" #ox ", %" #ax ", %" #wx "\n\tv_mul_f32_e64 %" #oy ", " N "%" #ax ", %" #wy "\n\t"
#define SF(ox, oy, ay, wx, wy, N) "v_fma_f32 %" #ox ", " N "%" #ay ", %" #wy ", %" #ox "\n\tv_fma_f32 %" #oy ", %" #ay ", %" #wx ", %" #oy "\n\t"
#define SC4(NM, NF) SM(0, 1, 8, 16, 17, NM) SM(2, 3, 10, 18, 19, NM) SM(4, 5, 12, 20, 21, NM) SM(6, 7, 14, 22, 23, NM) \
                    SF(0, 1, 9, 16, 17, NF) SF(2, 3, 11, 18, 19, NF) SF(4, 5, 13, 20, 21, NF) SF(6, 7, 15, 22, 23, NF)
#define SC3(NM, NF) SM(0, 1, 6, 12, 13, NM) SM(2, 3, 8, 14, 15, NM) SM(4, 5, 10, 16, 17, NM) \
                    SF(0, 1, 7, 12, 13, NF) SF(2, 3, 9, 14, 15, NF) SF(4, 5, 11, 16, 17, NF)
template <bool CONJ>
DI void cmul4_s(f2& a, f2& b, f2& c, f2& d, f2 wa, f2 wb, f2 wc, f2 wd) {
  float o[8];
#define SC4_OPS : "=&v"(o[0]), "=&v"(o[1]), "=&v"(o[2]), "=&v"(o[3]), "=&v"(o[4]), "=&v"(o[5]), "=&v"(o[6]), "=&v"(o[7]) \
      : "v"(a.x), "v"(a.y), "v"(b.x), "v"(b.y), "v"(c.x), "v"(c.y), "v"(d.x), "v"(d.y), "v"(wa.x), "v"(wa.y), "v"(wb.x), \
        "v"(wb.y), "v"(wc.x), "v"(wc.y), "v"(wd.x), "v"(wd.y)
  if constexpr (CONJ) asm(SC4("-", "") SC4_OPS); else asm(SC4("", "-") SC4_OPS);
  a = (f2){o[0], o[1]}; b = (f2){o[2], o[3]}; c = (f2){o[4], o[5]}; d = (f2){o[6], o[7]};
}
template <bool CONJ>
DI void cmul3_s(f2& a, f2& b, f2& c, f2 wa, f2 wb, f2 wc) {
  float o[6];
#define SC3_OPS : "=&v"(o[0]), "=&v"(o[1]), "=&v"(o[2]), "=&v"(o[3]), "=&v"(o[4]), "=&v"(o[5]) \
      : "v"(a.x), "v"(a.y), "v"(b.x), "v"(b.y), "v"(c.x), "v"(c.y), "v"(wa.x), "v"(wa.y), "v"(wb.x), "v"(wb.y), "v"(wc.x), "v"(wc.y)
  if constexpr (CONJ) asm(SC3("-", "") SC3_OPS); else asm(SC3("", "-") SC3_OPS);
  a = (f2){o[0], o[1]}; b = (f2){o[2], o[3]}; c = (f2){o[4], o[5]};
}
template <bool CONJ>
DI void twid7_s(f2 (&v)[8], const f2 (&t)[7]) {
  cmul4_s<CONJ>(v[1], v[2], v[3], v[4], t[0], t[1], t[2], t[3]);
  cmul3_s<CONJ>(v[5], v[6], v[7], t[4], t[5], t[6]);
}

DI void pin(f2& a) { asm volatile("" : "+v"(a)); }
DI void lgkm0() { asm volatile("s_waitcnt lgkmcnt(0)" ::: "memory"); }
DI void vm0() { asm volatile("s_waitcnt vmcnt(0)" ::: "memory"); }

template <bool PK, class H0, class H1>
DI void fft512_fwd(f2 (&v)[8], const f2 (&tw1)[7], const f2 (&tw2)[7], LAS f2* buf, int L, int lane, H0&& h0,
                   H1&& h1 ST_ARG) {
  if constexpr (PK) { dft8<1>(v); twid7<false>(v, tw1); } else { dft8_s<1>(v); twid7_s<false>(v, tw1); }
  lgkm0();
#pragma unroll
  for (int r = 0; r < 8; ++r) buf[72 * r + L] = v[r];
  {
    const int a = 72 * (lane >> 3) + (lane & 7);
    asm volatile("" ::: "memory");
#pragma unroll
    for (int r = 0; r < 8; ++r) v[r] = buf[a + 8 * r];
  }
  SB();
  h0();
  SB();
  if constexpr (PK) { dft8<1>(v); twid7<false>(v, tw2); } else { dft8_s<1>(v); twid7_s<false>(v, tw2); }
  lgkm0();
  {
    const int a = 66 * (lane & 7) + (lane >> 3);
#pragma unroll
    for (int r = 0; r < 8; ++r) buf[a + 8 * r] = v[r];
  }
  asm volatile("" ::: "memory");
#pragma unroll
  for (int r = 0; r < 8; ++r) v[r] = buf[66 * r + lane];
  SB();
  h1();
  SB();
  if constexpr (PK) dft8<1>(v); else dft8_s<1>(v);
}

template <bool PK>
DI void fft512_inv(f2 (&v)[8], const f2 (&tw1)[7], const f2 (&tw2)[7], LAS f2* buf, int L, int lane ST_ARG) {
  if constexpr (PK) dft8<-1>(v); else { dft8_s<-1>(v); }
  lgkm0();
#pragma unroll
  for (int r = 0; r < 8; ++r) buf[68 * r + lane] = v[r];
  asm volatile("" ::: "memory");
  {
    const int a = 68 * (lane & 7) + (lane >> 3);
#pragma unroll
    for (int r = 0; r < 8; ++r) v[r] = buf[a + 8 * r];
  }
  if constexpr (PK) { twid7<true>(v, tw2); dft8<-1>(v); } else { twid7_s<true>(v, tw2); dft8_s<-1>(v); }
  lgkm0();
  {
    const int a = 72 * (lane >> 3) + (lane & 7);
#pragma unroll
    for (int r = 0; r < 8; ++r) buf[a + 8 * r] = v[r];
  }
  asm volatile("" ::: "memory");
#pragma unroll
  for (int r = 0; r < 8; ++r) v[r] = buf[72 * r + L];
  if constexpr (PK) { twid7<true>(v, tw1); dft8<-1>(v); } else { twid7_s<true>(v, tw1); dft8_s<-1>(v); }
}

template <int CTRL>
DI float dppf(float v) {
  return __builtin_bit_cast(float, __builtin_amdgcn_update_dpp(0, __builtin_bit_cast(int, v), CTRL, 0xf, 0xf, true));
}
DI float half_sum(float v) {
  v += dppf<0xB1>(v);
  v += dppf<0x4E>(v);
  v += dppf<0x141>(v);
  v += dppf<0x140>(v);
  u32 b = __float_as_uint(v);
  auto r = __builtin_amdgcn_permlane16_swap(b, b, false, false);
  return __uint_as_float(r[0]) + __uint_as_float(r[1]);
}

DI f2 wave_sum2(f2 v) {
  auto r = __builtin_amdgcn_permlane32_swap(__float_as_uint(v.x), __float_as_uint(v.y), false, false);
  const u32 h = __float_as_uint(half_sum(__uint_as_float(r[0]) + __uint_as_float(r[1])));
  auto o = __builtin_amdgcn_permlane32_swap(h, h, false, false);
  return (f2){__uint_as_float(o[0]), __uint_as_float(o[1])};
}
DI f2 unbf(u32 p) { return (f2){bflo(p), bfhi(p)}; }
DI u32 pkbf2(f2 a) {
  u32 r;
  asm("v_cvt_pk_bf16_f32 %0, %1, %2" : "=v"(r) : "v"(a.x), "v"(a.y));
  return r;
}

DI f2 gelu2(f2 x) {
  const float c0 = -2.302208198144325f, c1 = -0.10294323959002349f;
  const f2 y = x * __builtin_elementwise_fma(x * x, (f2){c1, c1}, (f2){c0, c0});
  const f2 d = (f2){__builtin_amdgcn_exp2f(y.x), __builtin_amdgcn_exp2f(y.y)} + (f2){1.f, 1.f};
  return x * (f2){__builtin_amdgcn_rcpf(d.x), __builtin_amdgcn_rcpf(d.y)};
}

template <class T>
DI LAS T* lp(u32 a) { return (LAS T*)(uintptr_t)a; }
DI LAS u32* cw(int i) { return lp<u32>(L_CT + 4 * i); }
DI u32 l_get(int i) {
  return __builtin_amdgcn_readfirstlane(__hip_atomic_load(cw(i), __ATOMIC_RELAXED, __HIP_MEMORY_SCOPE_WORKGROUP));
}
DI void l_arrive(int i, int lane) {
  asm volatile("s_waitcnt lgkmcnt(0)\n\ts_mov_b64 exec, 1\n\tds_add_u32 %0, %1\n\ts_mov_b64 exec, -1" ::"v"(L_CT + 4 * i), "v"(1u) : "memory");
}
DI void l_set(int i, u32 v, int lane) {
  asm volatile("s_waitcnt lgkmcnt(0)\n\tds_write_b32 %0, %1" ::"v"(L_CT + 4 * i), "v"(v) : "memory");
}
template <bool SLEEP>
DI void l_wait(int i, u32 n) {
  u32 cnt, tmp, val;
  asm volatile(
      "s_mov_b32 %0, %4\n"
      ".Lw%=:\n\t"
      "ds_read_b32 %1, %3\n\t"
      "s_sub_u32 %0, %0, 1\n\t"
      "s_cbranch_scc1 .Le%=\n\t"
      "s_waitcnt lgkmcnt(0)\n\t"
      "v_readfirstlane_b32 %2, %1\n\t"
      "s_nop 0\n\t"
      "s_cmp_ge_u32 %2, %5\n\t"
      "s_cbranch_scc1 .Le%=\n\t"
      "s_sleep %6\n\t"
      "s_branch .Lw%=\n"
      ".Le%=:\n\t"
      "s_waitcnt lgkmcnt(0)"
      : "=&s"(cnt), "=&v"(tmp), "=&s"(val)
      : "v"(L_CT + 4 * i), "n"(SPIN_L), "s"(n), "n"(SLEEP ? 1 : 0)
      : "memory", "scc");
}
template <int SI = -1>
DI void publish(char* ws, int cnt, u32 n, u32 off, u32 rstride, int lane ST_ARG) {
  vm0();
  u32 old;
  asm volatile("s_mov_b64 exec, 1\n\tds_add_rtn_u32 %0, %1, %2\n\ts_mov_b64 exec, -1\n\ts_waitcnt lgkmcnt(0)"
               : "=&v"(old) : "v"(L_CT + 4 * cnt), "v"(1u) : "memory");
  old = __builtin_amdgcn_readfirstlane(old);
  if (old == n - 1) {
    const u32 tag = l_get(W_TAG);
    if (lane < REPS)
      __hip_atomic_store((u32*)(ws + off + lane * rstride), tag, __ATOMIC_RELAXED, __HIP_MEMORY_SCOPE_AGENT);
  }
  asm volatile("" ::: "memory");
}
template <class RS>
DI void sync_wait(char* ws, RS rs, u32 flags, u32 go, bool watcher, int lane) {
  const u32 tag = l_get(W_TAG);
  if (watcher) {
    bool ok = false;
#pragma nounroll
    for (int s = 0; s < SPIN_G; ++s) {
      asm volatile("" ::: "memory");
      const u32 g = __builtin_amdgcn_raw_buffer_load_b32(rs, 0, go, SC1);
      if (!ok) {
        const u4 v = __builtin_amdgcn_raw_buffer_load_b128(rs, flags + lane * 128, 0, SC1);
        ok = v[0] == tag && v[1] == tag && v[2] == tag && v[3] == tag;
      }
      if (__builtin_amdgcn_readfirstlane(g) == tag) break;
      if (__all(ok)) {
        if (lane < 8) __hip_atomic_store((u32*)(ws + (go & ~1023u) + lane * 128), tag, __ATOMIC_RELAXED, __HIP_MEMORY_SCOPE_AGENT);
        break;
      }
      __builtin_amdgcn_s_sleep(WATCH_SLEEP);
    }
  } else {
#pragma nounroll
    for (int s = 0; s < SPIN_G; ++s) {
      asm volatile("" ::: "memory");
      if (__builtin_amdgcn_readfirstlane(__builtin_amdgcn_raw_buffer_load_b32(rs, 0, go, SC1)) == tag) break;
      __builtin_amdgcn_s_sleep(POLL_SLEEP);
    }
  }
  asm volatile("" ::: "memory");
}

DI u4 mk_srd(const void* p, u32 bytes) {
  const uint64_t a = (uint64_t)p;
  u4 r;
  r[0] = __builtin_amdgcn_readfirstlane((u32)a);
  r[1] = __builtin_amdgcn_readfirstlane((u32)(a >> 32) & 0xffffu);
  r[2] = bytes;
  r[3] = 0x00020000u;
  return r;
}
template <bool COH>
DI void dma_r(u32 m0v, u32 v0, u32 v1, u4 srd, u32 so) {
  if constexpr (COH)
    asm volatile(
        "s_mov_b32 m0, %0\n\ts_nop 0\n\t"
        "buffer_load_dwordx4 %1, %3, %4 offen sc1 lds\n\t"
        "buffer_load_dwordx4 %2, %3, %4 offen offset:1024 sc1 lds"
        :
        : "s"(m0v), "v"(v0), "v"(v1), "s"(srd), "s"(so)
        : "memory");
  else
    asm volatile(
        "s_mov_b32 m0, %0\n\ts_nop 0\n\t"
        "buffer_load_dwordx4 %1, %3, %4 offen lds\n\t"
        "buffer_load_dwordx4 %2, %3, %4 offen offset:1024 lds"
        :
        : "s"(m0v), "v"(v0), "v"(v1), "s"(srd), "s"(so)
        : "memory");
}
DI void dma1(u32 m0v, u32 v, u4 srd, u32 so) {
  asm volatile("s_mov_b32 m0, %3\n\ts_nop 0\n\tbuffer_load_dwordx4 %0, %1, %2 offen lds" ::"v"(v), "s"(srd), "s"(so), "s"(m0v) : "memory");
}
DI void touch(u32 v, u4 srd, u32 so) {
  asm volatile("s_mov_b32 m0, %3\n\ts_nop 0\n\tbuffer_load_dwordx4 %0, %1, %2 offen lds" ::"v"(v), "s"(srd), "s"(so), "n"(L_JUNK) : "memory");
}
template <int OFF>
DI void ld_dw(u32& d, u32 v, u4 srd, u32 so) {
  asm volatile("buffer_load_dword %0, %1, %2, %3 offen offset:%4" : "=v"(d) : "v"(v), "s"(srd), "s"(so), "n"(OFF) : "memory");
}
DI void ld_p(u4& a, u4& b, u32 v, u4 srd, u32 so) {
  asm volatile(
      "buffer_load_dwordx4 %0, %2, %3, %4 offen sc1\n\t"
      "buffer_load_dwordx4 %1, %2, %3, %4 offen offset:1024 sc1"
      : "=&v"(a), "=&v"(b)
      : "v"(v), "s"(srd), "s"(so)
      : "memory");
}
template <int N>
DI void wait_p(u4& a, u4& b) {
  asm volatile("s_waitcnt vmcnt(%2)" : "+v"(a), "+v"(b) : "n"(N) : "memory");
}
template <int N>
DI void wait_vl() {
  asm volatile("s_waitcnt vmcnt(%0) lgkmcnt(0)" ::"n"(N) : "memory");
}
template <int OFF, int TOFF>
DI bf8 ld_tr(u32 a) {
  bf4 lo = __builtin_amdgcn_ds_read_tr16_b64_v4bf16(lp<bf4>(a + OFF));
  bf4 hi = __builtin_amdgcn_ds_read_tr16_b64_v4bf16(lp<bf4>(a + OFF + TOFF));
  return __builtin_shufflevector(lo, hi, 0, 1, 2, 3, 4, 5, 6, 7);
}
#define MFMA(a, b, c) __builtin_amdgcn_mfma_f32_16x16x32_bf16((a), (b), (c), 0, 0, 0)
template <int N>
DI void pin_acc(f4 (&a)[N]) {
#pragma unroll
  for (int i = 0; i < N; i += 4) asm volatile("" : "+v"(a[i]), "+v"(a[i + 1]), "+v"(a[i + 2]), "+v"(a[i + 3]));
}
DI bf8 asbf(u4 v) { return __builtin_bit_cast(bf8, v); }

DI constexpr u32 slot2(int j) { return (u32)((NR == 12 ? 0x13276540176540ull : NR == 14 ? 0x13765402176540ull : 0x17654032176540ull) >> (4 * j)) & 7; }

typedef __attribute__((ext_vector_type(8))) u32 u8v;
template <class T>
DI T* mkptr(u32 lo, u32 hi) { return (T*)(__attribute__((address_space(1))) T*)(((uint64_t)hi << 32) | lo); }

__global__ __launch_bounds__(512) void fused(const u32*, const u16*, const u16*, const u16*, const u32*, const u16*,
                                             const u16*, const u32*, char*, u16*) {
  extern __shared__ __attribute__((aligned(16))) char smem[];
  u8v ka, kb;
  u4 kc;
  auto args_load = [&] {
    asm volatile("s_load_dwordx8 %0, %3, 0x0\n\ts_load_dwordx8 %1, %3, 0x20\n\ts_load_dwordx4 %2, %3, 0x40"
                 : "=&s"(ka), "=&s"(kb), "=&s"(kc) : "s"(__builtin_amdgcn_kernarg_segment_ptr()));
  };
  const int tid = threadIdx.x, lane = tid & 63;
  const int w = __builtin_amdgcn_readfirstlane(tid >> 6);
  const int b = blockIdx.x, xg = b & 7, bi = b >> 3;
  const int fi = bi >> 3;
  const int dt = ARR2 ? 4 * (xg & 1) + (bi & 3) : bi & 7, st = ARR2 ? 8 * (xg >> 1) + (bi >> 2) : 4 * xg + fi;
  const bool isX = w >= 4;
  const bool hasTile = bi < 28;
  const bool special = b == 255;
  const int dt1 = (bi < 24 && !ARR1) ? bi & 7 : 4 * (xg & 1) + (bi & 3);
  const int ft = 7 * (xg >> 1) + (ARR1 ? (bi < 28 ? bi >> 2 : 0) : bi < 24 ? 4 * (xg & 1) + fi : 3);
  const int q = w & 3;
  const u32 fo = (b >> 2) * 128 + (b & 3) * 4;
  u4 sCode;
  {
    uint64_t pc;
    u32 len;
    asm volatile("s_getpc_b64 %0\n.Lcode_begin:\n\ts_mov_b32 %1, .Lcode_end-.Lcode_begin" : "=s"(pc), "=s"(len));
    sCode = (u4){(u32)pc, (u32)(pc >> 32) & 0xffffu, len, 0x00020000u};
  }
  auto warm_code = [&] { touch(lane * 16, sCode, bi * 1024); };
  if (w == 3) warm_code();
  if (w == 2) touch(lane * 16, sCode, bi * 1024 + 32768);
  STAMP_DECL
  STAMP(0);
  const int hh = lane >> 5, l5 = lane & 31;
  const int grp = xg * 32 + bi, s0 = grp * 8;
  const int cg = grp & 3, pg = grp >> 2, p0 = 32 * pg + 8 * q;
  const int L = 2 * l5 + hh;
  f2 tw1[7], tw2[7], tq[8], tf[2][3];
  auto twiddles_a = [&] {
#pragma unroll
    for (int r = 1; r < 8; ++r) {
      tw1[r - 1] = twid((L * r) & 511, 1.f / 512.f);
      tw2[r - 1] = twid(((lane & 7) * r) & 63, 1.f / 64.f);
    }
#pragma unroll
    for (int r = 0; r < 7; ++r) { pin(tw1[r]); pin(tw2[r]); }
  };
  auto twiddles_b = [&] {
#pragma unroll
    for (int r = 0; r < 8; ++r) tq[r] = twid(((64 * r + L) * q) & 2047, 1.f / 2048.f);
#pragma unroll
    for (int e = 0; e < 2; ++e)
#pragma unroll
      for (int j = 1; j < 4; ++j) tf[e][j - 1] = twid(((128 * q + 64 * e + lane) * j) & 2047, 1.f / 2048.f);
#pragma unroll
    for (int r = 0; r < 8; ++r) pin(tq[r]);
#pragma unroll
    for (int r = 0; r < 6; ++r) pin(tf[r / 3][r % 3]);
  };
  auto args_wait = [&] { asm volatile("s_waitcnt lgkmcnt(0)" : "+s"(ka), "+s"(kb), "+s"(kc)); };
#define x32 mkptr<const u32>(ka[0], ka[1])
#define wr mkptr<const u16>(ka[2], ka[3])
#define wi mkptr<const u16>(ka[4], ka[5])
#define w1 mkptr<const u16>(ka[6], ka[7])
#define b1 mkptr<const u32>(kb[0], kb[1])
#define w2 mkptr<const u16>(kb[2], kb[3])
#define b2 mkptr<const u16>(kb[4], kb[5])
#define gw32 mkptr<const u32>(kb[6], kb[7])
#define ws mkptr<char>(kc[0], kc[1])
#define out mkptr<u16>(kc[2], kc[3])
#define rsW __builtin_amdgcn_make_buffer_rsrc(ws, 0, WS_BYTES, 0x00020000)
#define sW2 mk_srd(w2, F_ * S_ * 2)
#define sWS mk_srd(ws, WS_BYTES)
  const u32 rr = lane >> 3, kq = lane >> 4, l4 = lane & 15;
  const u32 ldsb = q * WRING;
  const u32 rdc = ((l4 >> 2) * 128) + (((l4 >> 3) | ((kq & 1) << 1)) * 32) + ((lane >> 1) & 1) * 16 + 8 * (lane & 1);
  const u32 v2a = rr * 4096 + ((lane & 7) ^ (rr & 6)) * 16, v2b = v2a + 8 * 4096 - 1024;
  const u32 oW2 = 28 * q * 65536 + st * 128;

  const int rot2 = ((ARR2 ? 2 * ((7 * (bi & 3)) >> 1) : 2 * ((7 * dt) >> 2)) + (ROTS2 ? (ARR2 ? 2 * ((7 * (bi >> 2)) >> 2) : 2 * ((7 * fi) >> 1)) : 0)) % 28;
  auto k2pos = [&](int p) { const int k = p + rot2; return k >= 28 ? k - 28 : k; };
  auto issue2 = [&](int p) { dma_r<false>(ldsb + slot2(p >> 1) * 4096 + (p & 1) * 2048, v2a, v2b, sW2, oW2 + k2pos(p) * 65536); };
  u32 misc;
  auto ln_load = [&] {
    args_load();
    args_wait();
    const u4 sM = q == 1 ? mk_srd(b1, F_ * 2) : sWS;
    const u32 mo = q == 0 ? OFF_E : q == 1 ? (ft * 32 + l5) * 4 : OFF_FA + (q & 1) * 65536 + (xg % REPS) * 8192 + (2 * bi + (lane & 1)) * 128;
    asm volatile("s_barrier\n\tbuffer_load_dword %0, %1, %2, 0 offen sc1" : "=v"(misc) : "v"(mo), "s"(sM) : "memory");
    dma1(q == 0 ? L_GW : L_JUNK, lane * 16, mk_srd(gw32, q == 0 ? D_ * 2 : 0), 0);
  };
  auto ln_run = [&](auto&& mid) {
    l_wait<false>(F_XS + q, 1);
    STAMP(20);
    u32 gx[8];
#pragma unroll
    for (int j = 0; j < 8; ++j) gx[j] = *lp<u32>(L_XS + q * 2048 + hh * 1024 + j * 128 + l5 * 4);
    f2 xs[8];
#pragma unroll
    for (int j = 0; j < 8; ++j) {
      auto rx = __builtin_amdgcn_permlane32_swap(gx[j] << 16, gx[j] & 0xffff0000u, false, false);
      xs[j] = (f2){__uint_as_float(rx[0]), __uint_as_float(rx[1])};
    }
    const f2 mean = wave_sum2(((xs[0] + xs[1]) + (xs[2] + xs[3])) + ((xs[4] + xs[5]) + (xs[6] + xs[7]))) * (1.f / 512.f);
    STAMP(21);
    mid();
    f2 q0 = {0.f, 0.f}, q1 = {0.f, 0.f};
#pragma unroll
    for (int j = 0; j < 8; j += 2) {
      const f2 a = xs[j] - mean, c = xs[j + 1] - mean;
      q0 = __builtin_elementwise_fma(a, a, q0);
      q1 = __builtin_elementwise_fma(c, c, q1);
    }
    const f2 var = unbf(pkbf2(unbf(pkbf2(wave_sum2(q0 + q1) * (1.f / 512.f))) + 1e-5f));
    const f2 den = unbf(pkbf(__builtin_amdgcn_sqrtf(var.x), __builtin_amdgcn_sqrtf(var.y)));
    const f2 rinv = {__builtin_amdgcn_rcpf(den.x), __builtin_amdgcn_rcpf(den.y)};
    const f2 mb = unbf(pkbf2(mean));
#pragma unroll
    for (int j = 0; j < 8; ++j) *lp<u32>(L_HT + (64 * j + L) * 16 + q * 4) = pkbf2(unbf(pkbf2(xs[j] - mb)) * rinv);
    asm volatile("s_waitcnt vmcnt(8)" : "+v"(misc)::"memory");
    *lp<u32>(q == 0 ? L_CT + 4 * W_TAG : q == 1 ? L_B1 + l5 * 4 : L_CT + 4 * W_JUNK) = q == 0 ? misc * 0x9E3779B1u + 0x7F4A7C15u : misc;
    STAMP(23);
    l_arrive(C_G0, lane);
    l_wait<false>(C_G0, 4);
    STAMP(12);
    const int gt = tid & 255;
    const u4 c0 = *lp<u4>(L_HT + gt * 16), c1 = *lp<u4>(L_HT + 4096 + gt * 16);
    const u32 hp = OFF_HP + (s0 >> 5) * 32768 + ((s0 >> 3) & 3) * 256 + (gt >> 4) * 1024 + (gt & 15) * 16;
    __builtin_amdgcn_raw_buffer_store_b128(c0, rsW, hp, 0, SC1);
    __builtin_amdgcn_raw_buffer_store_b128(c1, rsW, hp + 16384, 0, SC1);
    STAMP(13);
    f2 d0 = {0.f, 0.f}, d1 = {0.f, 0.f};
    u32 gg[8];
#pragma unroll
    for (int j = 0; j < 8; ++j) gg[j] = *lp<u32>(L_GW + j * 128 + l5 * 4);
    const u32 gsh = 16 - 16 * hh;
#pragma unroll
    for (int j = 0; j < 8; j += 2) {
      const float g0 = __uint_as_float((gg[j] << gsh) & 0xffff0000u), g1 = __uint_as_float((gg[j + 1] << gsh) & 0xffff0000u);
      d0 += unbf(pkbf2(xs[j] * g0));
      d1 += unbf(pkbf2(xs[j + 1] * g1));
    }
    const f2 z = unbf(pkbf2(wave_sum2(d0 + d1)));
    float gate = rbf(__builtin_amdgcn_rcpf(1.f + __expf(-(lane & 1 ? z.y : z.x))));
    asm volatile("" : "+v"(gate));
    publish<19>(ws, C_PA, 8, OFF_FA + fo, 8192, lane ST_PASS);
    STAMP(14);
    __builtin_amdgcn_raw_buffer_store_b32(__float_as_uint(gate), rsW, lane < 2 ? OFF_G + (s0 + 2 * q + lane) * 4 : 0x80000000u, 0, SC1);
  };

  if (isX) {
    args_load();
    twiddles_a();
    args_wait();
    STAMP(24);
    __builtin_amdgcn_s_setprio(2);
    {
      const u4 sX = mk_srd(x32, S_ * D_ * 2);
      asm volatile("s_nop 4" ::: "memory");
      dma1(L_XS + q * 2048, lane * 16, sX, (s0 + 2 * q) * 1024);
      dma1(L_XS + q * 2048 + 1024, lane * 16, sX, (s0 + 2 * q + 1) * 1024);
    }
    u4 wre, wim;
    u32 nre, nim;
    {
      const u4 sR = mk_srd(wr, S_ * DF_ * 2), sI = mk_srd(wi, S_ * DF_ * 2);
      const u32 k10 = (p0 >> 9) + 4 * ((p0 & 511) >> 3);
      const u32 vc = rr * (256 * DF_ * 2) + cg * 128 + (lane & 7) * 16;
      const u32 vn = (cg == 0 && lane < 8) ? lane * (256 * DF_ * 2) + 512 : 0x80000000u;
      asm volatile("buffer_load_dwordx4 %0, %4, %6, %8 offen\n\tbuffer_load_dwordx4 %1, %4, %7, %8 offen\n\t"
                   "buffer_load_ushort %2, %5, %6, %8 offen\n\tbuffer_load_ushort %3, %5, %7, %8 offen"
                   : "=&v"(wre), "=&v"(wim), "=&v"(nre), "=&v"(nim)
                   : "v"(vc), "v"(vn), "s"(sR), "s"(sI), "s"(k10 * (DF_ * 2)) : "memory");
    }
    *lp<u32>(L_CT + 4 * lane + (u32)(uintptr_t)(LAS char*)smem) = 0;
    asm volatile("s_waitcnt lgkmcnt(0)\n\ts_barrier" ::: "memory");
    STAMP(20);
    asm volatile("s_waitcnt vmcnt(4)" ::: "memory");
    l_set(F_XS + q, 1, lane);
    const int xt = tid & 255;
    u32 xv[8];
#pragma unroll
    for (int j = 0; j < 8; ++j) xv[j] = *lp<u32>(L_XS + q * 2048 + hh * 1024 + j * 128 + l5 * 4);
    f2 v[8];
#pragma unroll
    for (int j = 0; j < 8; ++j) {
      auto rx = __builtin_amdgcn_permlane32_swap(xv[j] << 16, xv[j] & 0xffff0000u, false, false);
      v[j] = (f2){__uint_as_float(rx[0]), __uint_as_float(rx[1])};
    }
    STAMP(1);
    LAS f2* buf = lp<f2>(L_EX) + q * WS_;
    fft512_fwd<true>(v, tw1, tw2, buf, L, lane, [&] { STAMP(22); },
        [&] {
          STAMP(23);
          asm volatile("s_waitcnt vmcnt(0)" : "+v"(wre), "+v"(wim), "+v"(nre), "+v"(nim)::"memory");
          const u32 wa_ = L_WT + (lane & 7) * (8 * 144 + 16) + (8 * q + rr) * 4;
#pragma unroll
          for (int e = 0; e < 4; ++e) {
            *lp<u32>(wa_ + (2 * e) * 144) = __builtin_amdgcn_perm(wim[e], wre[e], 0x05040100u);
            *lp<u32>(wa_ + (2 * e + 1) * 144) = __builtin_amdgcn_perm(wim[e], wre[e], 0x07060302u);
          }
        } ST_PASS);
    lgkm0();
#pragma unroll
    for (int r = 0; r < 8; ++r) buf[64 * r + lane] = v[r];
    STAMP(2);
    l_arrive(C_X2, lane);
    l_wait<false>(C_X2, 4);
    STAMP(3);
    {
      const int j = xt & 7;
      LAS f2* zw = lp<f2>(L_EX) + (j >> 1) * WS_;
      const float sa = (j & 1) ? 0.f : 1.f, sb = (j & 1) ? 1.f : 0.f;
#pragma unroll
      for (int i = 0; i < 2; ++i) {
        const int c = (xt >> 3) + 32 * i;
        const f4 a01 = *(LAS f4*)(zw + 4 * c), a23 = *(LAS f4*)(zw + 4 * c + 2), c21 = *(LAS f4*)(zw + 510 - 4 * c);
        const f2 c0 = zw[(512 - 4 * c) & 511], c3 = zw[509 - 4 * c];
        auto sp = [&](float ax, float ay, float cx, float cy) {
          return (j & 1) ? pkbf(ay + cy, cx - ax) : pkbf(ax + cx, ay - cy);
        };
        const u4 o = {sp(a01.x, a01.y, c0.x, c0.y), sp(a01.z, a01.w, c21.z, c21.w), sp(a23.x, a23.y, c21.x, c21.y),
                      sp(a23.z, a23.w, c3.x, c3.y)};
        __builtin_amdgcn_raw_buffer_store_b128(o, rsW, OFF_T + c * 32768 + (s0 + j) * 16, 0, SC1);
      }
      const f2 za = zw[256];
      __builtin_amdgcn_raw_buffer_store_b32(pkbf(2.f * (sa * za.x + sb * za.y), 0.f), rsW, xt < 8 ? OFF_TN + (s0 + j) * 4 : 0x80000000u, 0, SC1);
      const u32 wr_ = L_WT + (xt >> 3) * 144 + (xt >> 6) * 16 + (xt & 7) * 16;
      const u4 t0 = *lp<u4>(wr_), t1 = *lp<u4>(wr_ + 32 * 144 + 64);
      const u32 wt = OFF_WT + ((64 * cg + (xt >> 3)) * 2048 + 32 * pg) * 4 + (xt & 7) * 16;
      __builtin_amdgcn_raw_buffer_store_b128(t0, rsW, wt, 0, SC1);
      __builtin_amdgcn_raw_buffer_store_b128(t1, rsW, wt + 32 * 8192, 0, SC1);
      __builtin_amdgcn_raw_buffer_store_b32(nre | nim << 16, rsW, (cg == 0 && lane < 8) ? OFF_WT + (256 * 2048 + p0 + lane) * 4 : 0x80000000u, 0, SC1);
    }
  } else if (hasTile) {
    ln_load();
    const u32 v1a = rr * (F_ * 2) + ((lane & 7) ^ (rr & 2)) * 16;
    const u32 v1b = rr * (F_ * 2) + ((lane & 7) ^ (rr & 2) ^ 4) * 16 + 8 * (F_ * 2) - 1024;
    const u4 sW1 = mk_srd(w1, F_ * S_ * 2);
    const u32 rd0 = (ldsb + rdc + kq * 1024) ^ ((q >> 1) * 64);
    const u32 rd[4] = {rd0, rd0 ^ 32, rd0 ^ 64, rd0 ^ 96};
    const int rot = ((bi < 24 && !ARR1) ? 4 * (bi & 7) : 8 * (bi & 3)) + (ROTS1 ? (ARR1 ? 2 * ((16 * (bi >> 2)) / 7) : 8 * fi) : 0);
    auto issue1 = [&](int p) {
      const u32 ks = 32 * q + ((p + rot) & 31);
      dma_r<false>(ldsb + (p & 15) * 2048, v1a, v1b, sW1, ks * (16 * F_ * 2) + ft * 128);
    };
    bf8 Rr[NR / 2][4];
    asm volatile("s_nop 4" ::: "memory");
    sfor<0, (SPLITW ? 2 : 4)>([&](auto ic) { issue1(ic.value); });
    ln_run([&] { if (SPLITW) sfor<2, 4>([&](auto ic) { issue1(ic.value); }); });
    sfor<0, 8>([&](auto ic) { Rr[ic.value >> 2][ic.value & 3] = ld_tr<(ic.value >> 2) * 4096, 512>(rd[ic.value & 3]); });
    l_wait<true>(C_PA, 8);
    STAMP(15);
    sfor<4, 20>([&](auto ic) { issue1(ic.value); });
    sfor<2, 8>([&](auto ic) {
      constexpr int j = ic.value;
      asm volatile("s_waitcnt vmcnt(28)" ::: "memory");
      sfor<0, 4>([&](auto jc) { Rr[j][jc.value] = ld_tr<j * 4096, 512>(rd[jc.value]); });
      lgkm0();
      issue1(16 + 2 * j);
      issue1(17 + 2 * j);
    });
    STAMP(1);
    l_wait<true>(GO1, GO1N);
    __builtin_amdgcn_s_setprio(GPRIO1);
    STAMP(2);
    f4 acc[16];
#pragma unroll
    for (int t = 0; t < 16; ++t) acc[t] = (f4){0.f, 0.f, 0.f, 0.f};
    u4 P[RS][2];
    bf8 Rl[2][4];
    const u32 vP = lane * 16;
    auto loadP = [&](int p, u4& a, u4& c) {
      const u32 ks = 16 * q + (((p + rot) & 31) >> 1);
      ld_p(a, c, vP, sWS, OFF_HP + ks * 32768 + dt1 * 4096 + ((p ^ q) & 1) * 2048);
    };
    sfor<0, PD>([&](auto ic) { loadP(ic.value, P[ic.value][0], P[ic.value][1]); });
    sfor<0, 32>([&](auto ic) {
      constexpr int i = ic.value, sl = i % RS, st_i = i >> 1, hf = i & 1;
      if constexpr (i == 1) STAMP(17);
      if constexpr (i == 12) STAMP(9);
      if constexpr (i == 22) STAMP(18);
      wait_p<2 * (PD - 1 < 31 - i ? PD - 1 : 31 - i) + 2 * (i > W2AT ? (i - W2AT < 4 ? i - W2AT : 4) : 0)>(P[sl][0], P[sl][1]);
      sfor<0, 8>([&](auto jc) {
        constexpr int j = jc.value, m = j >> 1, nn = j & 1, ai = (2 * (m >> 1) + hf) * 4 + nn * 2 + (m & 1);
        acc[ai] = MFMA(st_i < NR / 2 ? Rr[st_i % (NR / 2)][m] : Rl[st_i & 1][m], asbf(P[sl][nn]), acc[ai]);
        SB();
        if constexpr (j == 1 && i + PD < 32) loadP(i + PD, P[(i + PD) % RS][0], P[(i + PD) % RS][1]);
        if constexpr ((j == 2 || j == 3) && st_i + 1 >= NR / 2 && st_i < 15)
          Rl[(st_i + 1) & 1][2 * hf + j - 2] = ld_tr<((st_i + 1) & 7) * 4096, 512>(rd[2 * hf + j - 2]);
        if constexpr (j == 4 && i >= W2AT && i < W2AT + 4) issue2(i - W2AT);
        SB();
      });
    });
    pin_acc(acc);
    STAMP(3);
#pragma unroll
    for (int t = 4; t < 16; ++t) *lp<f4>(ldsb + t * 1024 + lane * 16) = acc[t];
    l_arrive(C_G1, lane);
    f2 s[8];
#pragma unroll
    for (int g = 0; g < 4; ++g) {
      const u2 bias = *lp<u2>(L_B1 + ((q >> 1) * 32 + (g & 1) * 16 + 4 * kq) * 2);
      s[2 * g] = acc[g].xy + unbf(bias.x);
      s[2 * g + 1] = acc[g].zw + unbf(bias.y);
    }
    l_wait<false>(C_G1, 4);
#pragma unroll
    for (int g = 0; g < 4; ++g) {
      const u32 a = g * 1024 + lane * 16;
      const f4 r = (*lp<f4>(a + (q ^ 1) * WRING + 4096) + *lp<f4>(a + (q ^ 2) * WRING + 8192)) + *lp<f4>(a + (q ^ 3) * WRING + 12288);
      s[2 * g] += r.xy;
      s[2 * g + 1] += r.zw;
    }
    l_arrive(C_G2, lane);
    u32 o[8];
#pragma unroll
    for (int g = 0; g < 8; ++g) {
      const f2 u = gelu2(s[g]);
      o[g] = pkbf(u.x, u.y);
    }
    const u32 up = OFF_UP + (ft * 2 + (q >> 1)) * 32768 + (dt1 * 4 + (q & 1) * 2) * 1024 + lane * 16;
    __builtin_amdgcn_raw_buffer_store_b128((u4){o[0], o[1], o[2], o[3]}, rsW, up, 0, SC1);
    __builtin_amdgcn_raw_buffer_store_b128((u4){o[4], o[5], o[6], o[7]}, rsW, up + 1024, 0, SC1);
    STAMP(4);
    publish<18>(ws, C_PB, 8, OFF_FB + fo, 8192, lane ST_PASS);
    STAMP(5);
    l_wait<true>(C_PB, 8);
  } else {
    ln_load();
    const u4 sW1 = mk_srd(w1, F_ * S_ * 2);
    const u32 v1a = rr * (F_ * 2) + (lane & 7) * 16;
    asm volatile("s_nop 4" ::: "memory");
    {
      u32 so = (512 * q + 128 * (bi & 3) + 64) * (F_ * 2) + ft * 128;
#pragma unroll
      for (int i = 0; i < 8; ++i) {
        touch(v1a, sW1, so);
        so += 8 * F_ * 2;
      }
    }
    ln_run([&] {});

    if (special) twiddles_a();
    if (!special) publish(ws, C_PB, 8, OFF_FB + fo, 8192, lane ST_PASS);
    STAMP(1);
  }

  if (isX || special) {
    u4 ga[4][4];
    const int col = isX ? grp : 256;
    LAS f2* zb = lp<f2>(isX ? L_EX : 0);
    const int zs = isX ? WS_ : (int)(WRING / 8);
    const int c3 = isX ? C_X3 : C_G3;
    __builtin_amdgcn_s_setprio(0);
    if (isX) {
      STAMP(15);
      publish<19>(ws, C_PA, 8, OFF_FA + fo, 8192, lane ST_PASS);
    }
    twiddles_b();
    if (isX) {
      STAMP(4);
#pragma unroll
      for (int m = 0; m < 4; ++m) {
        const int d = dt * 64 + (m ^ (q & 2)) * 16 + l4;
        f2 gz[4];
#pragma unroll
        for (int e = 0; e < 4; ++e) gz[e] = 2.f * twid(((64 * q + 2 * kq + (e & 1) + 8 * (e >> 1)) * d) & 511, 1.f / 512.f);
        f2 gr = twid((16 * d) & 511, 1.f / 512.f);
        asm volatile("s_nop 1" : "+v"(gr));
#pragma unroll
        for (int t = 0; t < 4; ++t) {
#pragma unroll
          for (int e = 0; e < 4; ++e) ga[t][m][e] = pkbf(gz[e].x, gz[e].y);
          if (t < 3) cmul4<false>(gz[0], gz[1], gz[2], gz[3], gr, gr, gr, gr);
        }
        ga[0][m][0] = (tid & 255) < 16 ? 0x3F803F80u | ((u32)lane << 31) : ga[0][m][0];
#pragma unroll
        for (int t = 0; t < 4; ++t) asm volatile("" : "+v"(ga[t][m]));
      }
      if (q == 1) {
        l_wait<true>(C_PA, 8);
        sync_wait(ws, rsW, OFF_FA + xg * 8192, OFF_GO + xg * 128, bi == 28 && xg < REPS, lane);
        l_set(F_A, 1, lane);
        if (b == 0 && lane == 0)
          __hip_atomic_store((u32*)(ws + OFF_E), l_get(W_TAG), __ATOMIC_RELAXED, __HIP_MEMORY_SCOPE_AGENT);
      }
    }
    l_wait<false>(F_A, 1);
    __builtin_amdgcn_s_setprio(XPRIO_B);
    STAMP(5);
    u32 tv[2][4];
    {
      const u32 tsh = __builtin_amdgcn_readfirstlane(isX ? 4 : 2);
      const u32 tb = __builtin_amdgcn_readfirstlane(isX ? OFF_T + (col >> 2) * 32768 + (col & 3) * 4 : OFF_TN);
#pragma unroll
      for (int e = 0; e < 2; ++e)
#pragma unroll
        for (int j = 0; j < 4; ++j)
          tv[e][j] = __builtin_amdgcn_raw_buffer_load_b32(rsW, lane << tsh, tb + ((128 * q + 512 * j + 64 * e) << tsh), SC1);
    }
    u4 wa = __builtin_amdgcn_raw_buffer_load_b128(rsW, OFF_WT + col * 8192 + q * 2048 + lane * 32, 0, SC1);
    u4 wb = __builtin_amdgcn_raw_buffer_load_b128(rsW, OFF_WT + col * 8192 + q * 2048 + lane * 32 + 16, 0, SC1);
    if (isX) l_arrive(C_TL, lane);
#pragma unroll
    for (int e = 0; e < 2; ++e) {
      f2 a[4];
#pragma unroll
      for (int j = 0; j < 4; ++j) a[j] = (f2){bflo(tv[e][j]), bfhi(tv[e][j])};
      const f2 p02 = a[0] + a[2], m02 = a[0] - a[2], p13 = a[1] + a[3], m13 = a[1] - a[3];
      const f2 r13 = (f2){m13.y, -m13.x};
      f2 y0 = p02 + p13, y1 = m02 + r13, y2 = p02 - p13, y3 = m02 - r13;
      cmul3<false>(y1, y2, y3, tf[e][0], tf[e][1], tf[e][2]);
      LAS f2* d = zb + 128 * q + 64 * e + lane;
      d[0] = y0;
      d[zs] = y1;
      d[2 * zs] = y2;
      d[3 * zs] = y3;
    }
    l_arrive(c3, lane);
    l_wait<false>(c3, 4);
    STAMP(6);
    CLK(c0);
    LAS f2* buf = zb + q * zs;
    f2 v[8];
#pragma unroll
    for (int r = 0; r < 8; ++r) v[r] = buf[64 * r + L];
    fft512_fwd<CPK>(v, tw1, tw2, buf, L, lane, [&] {}, [&] {} ST_PASS);
    if constexpr (CPK) {
      cmul4<false>(v[0], v[1], v[2], v[3], unbf(wa[0]), unbf(wa[1]), unbf(wa[2]), unbf(wa[3]));
      cmul4<false>(v[4], v[5], v[6], v[7], unbf(wb[0]), unbf(wb[1]), unbf(wb[2]), unbf(wb[3]));
    } else {
      cmul4_s<false>(v[0], v[1], v[2], v[3], unbf(wa[0]), unbf(wa[1]), unbf(wa[2]), unbf(wa[3]));
      cmul4_s<false>(v[4], v[5], v[6], v[7], unbf(wb[0]), unbf(wb[1]), unbf(wb[2]), unbf(wb[3]));
    }
    fft512_inv<CPK>(v, tw1, tw2, buf, L, lane ST_PASS);
    if constexpr (CPK) {
      cmul4<true>(v[0], v[1], v[2], v[3], tq[0], tq[1], tq[2], tq[3]);
      cmul4<true>(v[4], v[5], v[6], v[7], tq[4], tq[5], tq[6], tq[7]);
    } else {
      cmul4_s<true>(v[0], v[1], v[2], v[3], tq[0], tq[1], tq[2], tq[3]);
      cmul4_s<true>(v[4], v[5], v[6], v[7], tq[4], tq[5], tq[6], tq[7]);
    }
    lgkm0();
#pragma unroll
    for (int r = 0; r < 8; ++r) buf[(64 * r + L) ^ (((2 * r + (l5 >> 4)) & 3) << 1)] = v[r];
    l_arrive(c3 + 1, lane);
    l_wait<false>(c3 + 1, 4);
    CLK(c1);
    STAMP(7);
    {
      const float s2 = (q & 1) ? -1.f : 1.f;
      const float cx = (q & 1) ? 0.f : (q ? -1.f : 1.f), cy = (q & 1) ? (q == 1 ? 1.f : -1.f) : 0.f;
      u4 ore, oim;
#pragma unroll
      for (int c = 0; c < 4; ++c) {
        const int pos = 8 * lane + 2 * (c ^ ((lane >> 2) & 3));
        const f4 z0 = *(LAS f4*)(zb + pos), z1 = *(LAS f4*)(zb + zs + pos), z2 = *(LAS f4*)(zb + 2 * zs + pos),
                 z3 = *(LAS f4*)(zb + 3 * zs + pos);
        float y[4];
#pragma unroll
        for (int h = 0; h < 4; h += 2) {
          const float ox = fmaf(s2, z3[h], z1[h]), oy = fmaf(s2, z3[h + 1], z1[h + 1]);
          y[h] = fmaf(-cy, oy, fmaf(cx, ox, fmaf(s2, z2[h], z0[h])));
          y[h + 1] = fmaf(cy, ox, fmaf(cx, oy, fmaf(s2, z2[h + 1], z0[h + 1])));
          asm("" : "+v"(y[h]));
          asm("" : "+v"(y[h + 1]));
        }
        ore[c] = pkbf(y[0], y[2]);
        oim[c] = pkbf(y[1], y[3]);
      }
      if (!isX) l_arrive(C_G2, lane);
      const u32 yo = OFF_YT + (col == 256 ? 1 : 2 * col) * 4096 + q * 1024 + lane * 16;
      __builtin_amdgcn_raw_buffer_store_b128(ore, rsW, yo, 0, SC1);
      if (col != 0 && col != 256) __builtin_amdgcn_raw_buffer_store_b128(oim, rsW, yo + 4096, 0, SC1);
    }
    STAMP(8);
    publish<17>(ws, C_PB, 8, OFF_FB + fo, 8192, lane ST_PASS);
    STAMP(9);
   if (isX) {
    if (q == 1) {
      sync_wait(ws, rsW, OFF_FB + xg * 8192, OFF_GO + 1024 + xg * 128, bi == 28 && xg < REPS, lane);
      l_set(F_B, 1, lane);
    }
    l_wait<true>(F_B, 1);
    __builtin_amdgcn_s_sleep(YDELAY);
    STAMP(10);
    const u32 yb = L_EX + q * 6144;
    const u32 rd0 = (yb + rdc + kq * 512) ^ ((q & 1) * 64);
    const u32 rd[4] = {rd0, rd0 ^ 32, rd0 ^ 64, rd0 ^ 96};
    auto issueY = [&](int t) {
      dma_r<true>(yb + (t % 3) * 2048, v2a, v2b, sWS, OFF_YT + (8 * q + t) * 65536 + st * 128);
    };
    asm volatile("s_nop 4" ::: "memory");
    sfor<0, 3>([&](auto ic) { issueY(ic.value); });
    f4 acd[16];
#pragma unroll
    for (int t = 0; t < 16; ++t) acd[t] = (f4){0.f, 0.f, 0.f, 0.f};
    bf4 yh[2][4];
    sfor<0, 8>([&](auto ic) {
      constexpr int t = ic.value;
      asm volatile("s_waitcnt vmcnt(%0)" ::"n"(t < 6 ? 4 : 14 - 2 * t) : "memory");
      sfor<0, 4>([&](auto jc) { yh[t & 1][jc.value] = __builtin_amdgcn_ds_read_tr16_b64_v4bf16(lp<bf4>(rd[jc.value] + (t % 3) * 2048)); });
      if constexpr (t + 3 < 8) {
        lgkm0();
        issueY(t + 3);
      }
      if constexpr (t & 1)
        sfor<0, 16>([&](auto jc) {
          constexpr int m = jc.value >> 2, n = jc.value & 3, ai = (2 * (m >> 1) + (n >> 1)) * 4 + (n & 1) * 2 + (m & 1);
          acd[ai] = MFMA(asbf(ga[t >> 1][m]), __builtin_shufflevector(yh[0][n], yh[1][n], 0, 1, 2, 3, 4, 5, 6, 7), acd[ai]);
        });
    });
    pin_acc(acd);
    STAMP(12);
    if (q == 0) {
      const int s = st * 64 + lane;
      const float g = __uint_as_float(__builtin_amdgcn_raw_buffer_load_b32(rsW, OFF_G + s * 4, 0, SC1));
      const float og = rbf(1.f - g);
      *lp<float>(L_CF + lane * 4) = og;
      *lp<float>(L_CF + 256 + lane * 4) = g * (1.f / 2097152.f);
      *lp<float>(L_CF + 512 + lane * 4) = bflo(b2[s]);
    }
    static_assert(NR == 16, "slots 2 and 3 of the ring must be idle during GEMM2");
    l_wait<true>(F_FREE + q, 1);
#pragma unroll
    for (int g = 4; g < 12; ++g) *lp<f4>(ldsb + 4096 + g * 1024 + lane * 16) = acd[g];
    l_arrive(C_XR, lane);
    l_wait<XSL>(C_XR, 4);
#pragma unroll
    for (int g = 0; g < 4; ++g) *lp<f4>(L_EX + q * 4096 + g * 1024 + lane * 16) = acd[12 + g];
    l_arrive(C_XR, lane);
    f4 pa[4], pb[4];
#pragma unroll
    for (int g = 0; g < 4; ++g) {
      pa[g] = *lp<f4>((q ^ 1) * WRING + 8192 + g * 1024 + lane * 16);
      pb[g] = *lp<f4>((q ^ 2) * WRING + 12288 + g * 1024 + lane * 16);
    }
    l_wait<XSL>(C_XR, 8);
#pragma unroll
    for (int g = 0; g < 4; ++g) {
      const f4 pc = *lp<f4>(L_EX + (q ^ 3) * 4096 + g * 1024 + lane * 16);
#pragma unroll
      for (int e = 0; e < 4; ++e) {
        float t;
        asm("v_add_f32 %0, %1, %2\n\tv_add_f32 %0, %0, %3\n\tv_add_f32 %0, %0, %4" : "=&v"(t) : "v"(acd[g][e]), "v"(pa[g][e]), "v"(pb[g][e]), "v"(pc[e]));
        acd[g][e] = t;
      }
    }
    l_arrive(C_XR, lane);
    l_wait<XSL>(C_XR, 12);
#pragma unroll
    for (int g = 0; g < 4; ++g)
      *lp<f4>(L_EX + ((q & 1) * 32 + (g >> 1) * 16 + l4) * 256 + ((((q >> 1) * 2 + (g & 1)) * 4 + kq) ^ (l4 & 7)) * 16) = acd[g];
    STAMP(11);
   }
  }
  if (!isX) {
    const u32 rd0 = ldsb + rdc + kq * 512;
    const u32 rd[4] = {rd0, rd0 ^ 32, rd0 ^ 64, rd0 ^ 96};
    if (!hasTile) {
      l_wait<true>(C_X3, 4);
      if (special) l_wait<false>(C_G2, 4);
      asm volatile("s_nop 4" ::: "memory");
      sfor<0, 4>([&](auto ic) { issue2(ic.value); });
      vm0();
    }
    bf8 Rr[NR / 2][4];
    sfor<0, 8>([&](auto ic) { Rr[ic.value >> 2][ic.value & 3] = ld_tr<slot2(ic.value >> 2) * 4096, 2048>(rd[ic.value & 3]); });
    sfor<4, 16>([&](auto ic) { issue2(ic.value); });
    lgkm0();
    sfor<16, 20>([&](auto ic) { issue2(ic.value); });
    sfor<2, 8>([&](auto ic) {
      constexpr int j = ic.value;
      asm volatile("s_waitcnt vmcnt(%0)" ::"n"(j < 7 ? 28 : 24) : "memory");
      sfor<0, 4>([&](auto jc) { Rr[j][jc.value] = ld_tr<slot2(j) * 4096, 2048>(rd[jc.value]); });
      if constexpr (j < 6) {
        lgkm0();
        issue2(16 + 2 * j);
        issue2(17 + 2 * j);
      }
    });
    l_set(F_FREE + q, 1, lane);
    STAMP(6);
    l_wait<true>(F_B, 1);
    __builtin_amdgcn_s_setprio(GPRIO2);
    STAMP(7);
    f4 acc[16];
#pragma unroll
    for (int t = 0; t < 16; ++t) acc[t] = (f4){0.f, 0.f, 0.f, 0.f};
    u4 P[RS][2];
    bf8 Rl[2][4];
    const u32 vP = lane * 16;
    const u32 oP = OFF_UP + 14 * q * 32768 + dt * 4096;
    auto loadP = [&](int p, u4& a, u4& c) { ld_p(a, c, vP, sWS, oP + (k2pos(p) >> 1) * 32768 + (p & 1) * 2048); };
    sfor<0, PD>([&](auto ic) { loadP(ic.value, P[ic.value][0], P[ic.value][1]); });
    sfor<0, 28>([&](auto ic) {
      constexpr int i = ic.value, sl = i % RS, st_i = i >> 1, hf = i & 1;
      if constexpr (i == 1) STAMP(16);
      if constexpr (i == 12) STAMP(8);
      wait_p<2 * (PD - 1 < 27 - i ? PD - 1 : 27 - i)>(P[sl][0], P[sl][1]);
      sfor<0, 8>([&](auto jc) {
        constexpr int j = jc.value, mm = j >> 2, n = j & 3, ai = (2 * hf + mm) * 4 + n;
        acc[ai] = MFMA(asbf(P[sl][mm]), st_i < NR / 2 ? Rr[st_i % (NR / 2)][n] : Rl[st_i & 1][n], acc[ai]);
        SB();
        if constexpr (j == 1 && i + PD < 28) loadP(i + PD, P[(i + PD) % RS][0], P[(i + PD) % RS][1]);
        if constexpr ((j == 2 || j == 3) && st_i + 1 >= NR / 2 && st_i < 13)
          Rl[(st_i + 1) & 1][2 * hf + j - 2] = ld_tr<slot2(st_i + 1) * 4096, 2048>(rd[2 * hf + j - 2]);
        SB();
      });
    });
    pin_acc(acc);
    STAMP(10);
#pragma unroll
    for (int t = 0; t < 16; ++t)
      *lp<f4>(ldsb + 16384 + ((t & 3) * 16 + l4) * 256 + (((t >> 2) * 4 + kq) ^ (l4 & 7)) * 16) = acc[t];
  }
  lgkm0();
  __builtin_amdgcn_s_barrier();
  asm volatile("" ::: "memory");
  {
    const int r = tid >> 3;
    const float og = *lp<float>(L_CF + r * 4), gs = *lp<float>(L_CF + 256 + r * 4), bb = *lp<float>(L_CF + 512 + r * 4);
    u32 o[4];
#pragma unroll
    for (int c = 0; c < 2; ++c) {
      const u32 a = r * 256 + (((tid & 7) * 2 + c) ^ (r & 7)) * 16;
      const f4 sb = (*lp<f4>(a + 16384) + *lp<f4>(a + 16384 + WRING)) + (*lp<f4>(a + 16384 + 2 * WRING) + *lp<f4>(a + 16384 + 3 * WRING));
      const f4 sa = *lp<f4>(a + L_EX);
      const f4 v = (sb + bb) * og + sa * gs;
      o[2 * c] = pkbf(v.x, v.y);
      o[2 * c + 1] = pkbf(v.z, v.w);
    }
    *(u4*)(out + (size_t)(st * 64 + r) * D_ + dt * 64 + (tid & 7) * 8) = (u4){o[0], o[1], o[2], o[3]};
  }
  asm volatile(".Lcode_end:" ::: "memory");
  STAMP_FLUSH();
}

#undef x32
#undef wr
#undef wi
#undef w1
#undef b1
#undef w2
#undef b2
#undef gw32
#undef ws
#undef out
#undef rsW
#undef sW2
#undef sWS

extern "C" void kernel_launch(void* const* d_in, const int* in_sizes, int n_in, void* d_out, int out_size,
                              void* d_ws, size_t ws_size, hipStream_t stream) {
  (void)hipFuncSetAttribute((const void*)fused, hipFuncAttributeMaxDynamicSharedMemorySize, LDS_BYTES);
  fused<<<256, 512, LDS_BYTES, stream>>>((const u32*)d_in[0], (const u16*)d_in[1], (const u16*)d_in[2],
                                         (const u16*)d_in[4], (const u32*)d_in[5], (const u16*)d_in[6],
                                         (const u16*)d_in[7], (const u32*)d_in[8], (char*)d_ws, (u16*)d_out);
}
```
